```python
import torch

def _softmax(x, axis=-1):
    return torch.softmax(x, dim=axis)

def _gelu(x):
    return torch.nn.functional.gelu(x, approximate='tanh')

def _silu(x):
    return torch.nn.functional.silu(x)

def _sigmoid(x):
    return torch.sigmoid(x)

def _layernorm(x, axis=-1, eps=1e-05):
    m = x.mean(dim=axis, keepdim=True)
    v = x.var(dim=axis, keepdim=True, unbiased=False)
    return (x - m) / torch.sqrt(v + eps)

def _scatter_add(tgt, idx, src, axis=0):
    return tgt.clone().index_add_(axis, idx, src)

def _segment_sum(src, seg, n):
    out = torch.zeros((n,) + src.shape[1:], dtype=src.dtype)
    return out.index_add_(0, seg, src)

def _pad(x, pw):
    flat = tuple((v for pair in reversed(pw) for v in pair))
    return torch.nn.functional.pad(x, flat)
kernel_name = 'adaLN_cond__linear_kernel_hedgehog__mixer_gmlp__prompt'

def setup_inputs():

    _w = lambda a: (lambda t: t.to(torch.bfloat16) if t.is_floating_point() else t)(torch.as_tensor(a))

    def _A_setup():
        B, S, D, H = (1, 2048, 1024, 32)
        Dh = D // H
        return {'x': _w((lambda *s: torch.randn(*s, dtype=torch.bfloat16) * 0.05)(B, S, D)), 'wqkv': _w((lambda *s: torch.randn(*s, dtype=torch.bfloat16) * 0.4)(D, 3 * D)), 'w_phi': _w((lambda *s: torch.randn(*s, dtype=torch.bfloat16) * 0.4)(H, Dh, Dh) * Dh ** (-0.5)), 'wo': _w((lambda *s: torch.randn(*s, dtype=torch.bfloat16) * 0.05)(D, D))}
    _A_in = _A_setup()

    def _B_setup():
        B, S, D, F = (1, 2048, 1024, 3584)
        Fh = F // 2
        return {'x': _w((lambda *s: torch.randn(*s, dtype=torch.bfloat16) * 0.05)(B, S, D)), 'w_in': _w((lambda *s: torch.randn(*s, dtype=torch.bfloat16) * 0.2)(D, 2 * Fh)), 'w_sp': _w(0.01 * (lambda *s: torch.randn(*s, dtype=torch.bfloat16) * 0.2)(S, S)), 'b_sp': _w(1.0 + 0.01 * (lambda *s: torch.randn(*s, dtype=torch.bfloat16) * 0.05)(S)), 'w_out': _w((lambda *s: torch.randn(*s, dtype=torch.bfloat16) * 0.05)(Fh, D))}
    _B_in = _B_setup()
    return {'A_x': _A_in['x'], 'A_wqkv': _A_in['wqkv'], 'A_w_phi': _A_in['w_phi'], 'A_wo': _A_in['wo'], 'B_x': _B_in['x'], 'B_w_in': _B_in['w_in'], 'B_w_sp': _B_in['w_sp'], 'B_b_sp': _B_in['b_sp'], 'B_w_out': _B_in['w_out'], 'c': _w((lambda *s: torch.randn(*s, dtype=torch.bfloat16) * 0.1)(1, 1024) * 8.0), 'w_mod': _w((lambda *s: torch.randn(*s, dtype=torch.bfloat16) * 0.1)(1024, 6 * 1024) * 1024 ** (-0.5) * 8.0)}

def reference(A_x, A_wqkv, A_w_phi, A_wo, B_x, B_w_in, B_w_sp, B_b_sp, B_w_out, c, w_mod):

    def _A_ref(x, wqkv, w_phi, wo):
        B, S, D = x.shape
        H = 32
        Dh = D // H
        qkv = (lambda x, *d: x.permute(*d))((x @ wqkv).reshape(B, S, 3, H, Dh), 2, 0, 3, 1, 4)
        q, k, v = (qkv[0], qkv[1], qkv[2])
        pq = torch.einsum('bhsd,hdf->bhsf', q, w_phi)
        pk = torch.einsum('bhsd,hdf->bhsf', k, w_phi)
        phi_q = torch.concatenate([_softmax(pq, axis=-1), _softmax(-pq, axis=-1)], axis=-1)
        phi_k = torch.concatenate([_softmax(pk, axis=-1), _softmax(-pk, axis=-1)], axis=-1)
        kv = torch.einsum('bhsf,bhsd->bhfd', phi_k, v)
        z = torch.einsum('bhsf->bhf', phi_k)
        num = torch.einsum('bhqf,bhfd->bhqd', phi_q, kv)
        den = torch.einsum('bhqf,bhf->bhq', phi_q, z)[..., None] + 1e-06
        o = num / den
        return (lambda x, *d: x.permute(*d))(o, 0, 2, 1, 3).reshape(B, S, D) @ wo

    def _B_ref(x, w_in, w_sp, b_sp, w_out):
        B, S, D = x.shape
        F = w_in.shape[-1]
        Fh = F // 2
        z = _gelu(torch.einsum('bsd,df->bsf', x, w_in))
        z1 = z[..., :Fh].clone()
        z2 = _layernorm(z[..., Fh:], axis=-1)
        g = torch.einsum('ts,bsf->btf', w_sp, z2) + b_sp[None, :, None]
        return torch.einsum('bsf,fd->bsd', z1 * g, w_out)
    x = A_x
    mod = (_silu(c) @ w_mod).reshape(c.shape[0], 6, 1, -1)
    sa, ca, ga, sb, cb, gb = (mod[:, 0].clone(), mod[:, 1].clone(), mod[:, 2].clone(), mod[:, 3].clone(), mod[:, 4].clone(), mod[:, 5].clone())
    h = _layernorm(x, axis=-1) * (1 + ca) + sa
    x = x + ga * _A_ref(h, A_wqkv, A_w_phi, A_wo)
    h = _layernorm(x, axis=-1) * (1 + cb) + sb
    x = x + gb * _B_ref(h, B_w_in, B_w_sp, B_b_sp, B_w_out)
    return x
```

```cpp
#include <hip/hip_runtime.h>
#include <hip/hip_bf16.h>
#include <stdint.h>
typedef unsigned short u16;
__device__ __forceinline__ float bf2f(u16 b){ return __uint_as_float(((unsigned)b)<<16); }
__device__ __forceinline__ u16 f2bf(float f){ unsigned u=__float_as_uint(f); u+=0x7fffu+((u>>16)&1u); return (u16)(u>>16);}
#define ROUND 1
__device__ __forceinline__ float rb(float f){
  return bf2f(f2bf(f));
}
constexpr int S=2048,D=1024,H=32,DH=32,FH=1792;

template<typename TA, typename TB>
__global__ void gemm_naive(const TA* A,int lda,const TB* B,int ldb,float* C,int ldc,int M,int N,int K){
  __shared__ float As[32][33], Bs[32][33];
  int tx=threadIdx.x, ty=threadIdx.y; int row=blockIdx.y*32+ty, col=blockIdx.x*32+tx; float acc=0;
  for(int k0=0;k0<K;k0+=32){
    if constexpr(sizeof(TA)==2) As[ty][tx]=bf2f(A[(size_t)row*lda+k0+tx]); else As[ty][tx]=A[(size_t)row*lda+k0+tx];
    if constexpr(sizeof(TB)==2) Bs[ty][tx]=bf2f(B[(size_t)(k0+ty)*ldb+col]); else Bs[ty][tx]=B[(size_t)(k0+ty)*ldb+col];
    __syncthreads();
    for(int k=0;k<32;++k) acc+=As[ty][k]*Bs[k][tx];
    __syncthreads();
  }
  C[(size_t)row*ldc+col]=rb(acc);
}
__global__ void k_mod(const u16* c,const u16* w,float* mod){
  int n=blockIdx.x*blockDim.x+threadIdx.x; float acc=0;
  for(int k=0;k<D;++k){ float cv=bf2f(c[k]); float s=rb(cv/(1.f+__expf(-cv))); acc+=s*bf2f(w[(size_t)k*6144+n]); }
  mod[n]=rb(acc);
}
template<typename T>
__global__ void k_ln(const T* in,int ld,int n,const float* sc,const float* sh,float* out,int ldo){
  __shared__ float red[256]; int r=blockIdx.x; int t=threadIdx.x;
  auto ld1=[&](int i)->float{ if constexpr(sizeof(T)==2) return bf2f(in[(size_t)r*ld+i]); else return in[(size_t)r*ld+i]; };
  float s=0; for(int i=t;i<n;i+=256) s+=ld1(i);
  red[t]=s; __syncthreads(); for(int o=128;o;o>>=1){ if(t<o) red[t]+=red[t+o]; __syncthreads(); }
  float m=red[0]/n; __syncthreads();
  float q=0; for(int i=t;i<n;i+=256){ float d=ld1(i)-m; q+=d*d; }
  red[t]=q; __syncthreads(); for(int o=128;o;o>>=1){ if(t<o) red[t]+=red[t+o]; __syncthreads(); }
  float v=rb(red[0]/n); float mb=rb(m); float sd=rb(sqrtf(rb(v+1e-5f)));
  for(int i=t;i<n;i+=256){ float y=rb(rb(ld1(i)-mb)/sd); if(sc){ y=rb(rb(y*rb(1.f+sc[i]))+sh[i]); } out[(size_t)r*ldo+i]=y; }
}
__global__ void k_phi(const float* qkv,int off,const u16* wphi,float* phi){
  int s=blockIdx.x, h=threadIdx.y, f=threadIdx.x;
  float acc=0; for(int d=0;d<DH;++d) acc+=qkv[(size_t)s*3072+off+h*DH+d]*bf2f(wphi[(h*DH+d)*DH+f]);
  acc=rb(acc);
  float mx=acc, mn=acc; for(int o=16;o;o>>=1){ mx=fmaxf(mx,__shfl_xor(mx,o,32)); mn=fminf(mn,__shfl_xor(mn,o,32)); }
  float e1=expf(acc-mx), e2=expf(mn-acc); float s1=e1,s2=e2; for(int o=16;o;o>>=1){ s1+=__shfl_xor(s1,o,32); s2+=__shfl_xor(s2,o,32);}
  phi[((size_t)h*S+s)*64+f]=rb(e1/s1); phi[((size_t)h*S+s)*64+32+f]=rb(e2/s2);
}
__global__ void k_kv(const float* phik,const float* qkv,float* kv,float* z){
  int h=blockIdx.x, f=blockIdx.y, d=threadIdx.x;
  float acc=0;
  if(d<32){ for(int s=0;s<S;++s) acc+=phik[((size_t)h*S+s)*64+f]*qkv[(size_t)s*3072+2048+h*DH+d]; kv[(h*64+f)*32+d]=rb(acc);}
  else if(d==32){ for(int s=0;s<S;++s) acc+=phik[((size_t)h*S+s)*64+f]; z[h*64+f]=rb(acc);}
}
__global__ void k_o(const float* phiq,const float* kv,const float* z,float* o){
  int s=blockIdx.x,h=threadIdx.y,d=threadIdx.x; float num=0,den=0;
  for(int f=0;f<64;++f){ float p=phiq[((size_t)h*S+s)*64+f]; num+=p*kv[(h*64+f)*32+d]; den+=p*z[h*64+f]; }
  num=rb(num); den=rb(rb(den)+1e-6f); o[(size_t)s*D+h*DH+d]=rb(num/den);
}
__global__ void k_res1(const u16* x,const float* a,const float* g,float* x1){ int i=blockIdx.x*256+threadIdx.x; x1[i]=rb(bf2f(x[i])+rb(g[i%D]*a[i])); }
__global__ void k_gelu(float* z,int n){ int i=blockIdx.x*256+threadIdx.x; float v=z[i]; float t=tanhf(0.7978845608028654f*(v+0.044715f*v*v*v)); z[i]=rb(0.5f*v*(1.f+t)); }
__global__ void k_gate(const float* z,const float* g,const u16* b,float* u){ int i=blockIdx.x*256+threadIdx.x; int t=i/FH,f=i%FH; u[i]=rb(z[(size_t)t*3584+f]*rb(g[i]+bf2f(b[t]))); }
__global__ void k_res2(const float* x1,const float* a,const float* g,u16* out){ int i=blockIdx.x*256+threadIdx.x; out[i]=f2bf(x1[i]+rb(g[i%D]*a[i])); }

extern "C" void kernel_launch(void* const* d_in, const int* in_sizes, int n_in, void* d_out, int out_size, void* d_ws, size_t ws_size, hipStream_t stream) {
  const u16* x=(const u16*)d_in[0]; const u16* wqkv=(const u16*)d_in[1]; const u16* wphi=(const u16*)d_in[2]; const u16* wo=(const u16*)d_in[3];
  const u16* w_in=(const u16*)d_in[5]; const u16* w_sp=(const u16*)d_in[6]; const u16* b_sp=(const u16*)d_in[7]; const u16* w_out=(const u16*)d_in[8];
  const u16* c=(const u16*)d_in[9]; const u16* w_mod=(const u16*)d_in[10];
  float* p=(float*)d_ws; auto al=[&](size_t n){ float* r=p; p+=n; return r; };
  float* mod=al(8192); float* h=al((size_t)S*D); float* qkv=al((size_t)S*3072); float* phiq=al((size_t)H*S*64); float* phik=al((size_t)H*S*64);
  float* kv=al(H*64*32); float* z=al(H*64); float* o=al((size_t)S*D); float* a=al((size_t)S*D); float* x1=al((size_t)S*D);
  float* zz=al((size_t)S*3584); float* z2=al((size_t)S*FH); float* g=al((size_t)S*FH); float* u=al((size_t)S*FH);
  dim3 b2(32,32);
  k_mod<<<6144/256,256,0,stream>>>(c,w_mod,mod);
  k_ln<u16><<<S,256,0,stream>>>(x,D,D,mod+1024,mod,h,D);
  gemm_naive<float,u16><<<dim3(3072/32,S/32),b2,0,stream>>>(h,D,wqkv,3072,qkv,3072,S,3072,D);
  k_phi<<<S,b2,0,stream>>>(qkv,0,wphi,phiq);
  k_phi<<<S,b2,0,stream>>>(qkv,1024,wphi,phik);
  k_kv<<<dim3(H,64),64,0,stream>>>(phik,qkv,kv,z);
  k_o<<<S,b2,0,stream>>>(phiq,kv,z,o);
  gemm_naive<float,u16><<<dim3(D/32,S/32),b2,0,stream>>>(o,D,wo,D,a,D,S,D,D);
  k_res1<<<S*D/256,256,0,stream>>>(x,a,mod+2048,x1);
  k_ln<float><<<S,256,0,stream>>>(x1,D,D,mod+4096,mod+3072,h,D);
  gemm_naive<float,u16><<<dim3(3584/32,S/32),b2,0,stream>>>(h,D,w_in,3584,zz,3584,S,3584,D);
  k_gelu<<<S*3584/256,256,0,stream>>>(zz,S*3584);
  k_ln<float><<<S,256,0,stream>>>(zz+FH,3584,FH,nullptr,nullptr,z2,FH);
  gemm_naive<u16,float><<<dim3(FH/32,S/32),b2,0,stream>>>(w_sp,S,z2,FH,g,FH,S,FH,S);
  k_gate<<<S*FH/256,256,0,stream>>>(zz,g,b_sp,u);
  gemm_naive<float,u16><<<dim3(D/32,S/32),b2,0,stream>>>(u,FH,w_out,D,a,D,S,D,FH);
  k_res2<<<S*D/256,256,0,stream>>>(x1,a,mod+5120,(u16*)d_out);
}
```

```cpp
#include <hip/hip_runtime.h>
#include <stdint.h>

typedef unsigned short u16;
typedef __bf16 bf16x8 __attribute__((ext_vector_type(8)));
typedef __bf16 bf16x2 __attribute__((ext_vector_type(2)));
typedef short s16x4 __attribute__((ext_vector_type(4)));
typedef float f32x2 __attribute__((ext_vector_type(2)));
typedef float f32x4 __attribute__((ext_vector_type(4)));
typedef unsigned u32x4 __attribute__((ext_vector_type(4)));
typedef unsigned u32x2 __attribute__((ext_vector_type(2)));
#define LAS __attribute__((address_space(3)))
#define GAS __attribute__((address_space(1)))
#define DI __device__ __forceinline__
#define SB() __builtin_amdgcn_sched_barrier(0)
#define RLX_AGENT __ATOMIC_RELAXED, __HIP_MEMORY_SCOPE_AGENT
#define UNR _Pragma("unroll")

constexpr int S_ = 2048, D_ = 1024, FH = 1792;
constexpr size_t WS_CNT  = 0;
constexpr size_t WS_MODA = 32768;
constexpr size_t WS_MODB = WS_MODA + 65536;
constexpr size_t WS_MODF = WS_MODB + 65536;
constexpr size_t WS_CSB  = WS_MODF + 16384;
constexpr size_t WS_ZP   = WS_CSB + 32768;
constexpr size_t WS_ST1  = WS_ZP + 131072;
constexpr size_t WS_ST2  = WS_ST1 + 262144;
constexpr size_t WS_KVP  = 1u << 20;
constexpr size_t WS_H    = WS_KVP + (2u << 20);
constexpr size_t WS_O    = WS_H + (4u << 20);
constexpr size_t WS_X1   = WS_O + (4u << 20);
constexpr size_t WS_Y    = WS_X1 + (4u << 20);
constexpr size_t WS_Z2N  = WS_Y + (4u << 20);
constexpr size_t WS_U    = WS_Z2N + (8u << 20);
constexpr size_t WS_END  = WS_U + (8u << 20);
static_assert(WS_ST2 + 14 * 2048 * 16 <= WS_KVP, "ws map");
constexpr int C_KV = 0, C_O = 16, C_Y = 32, C_S2 = 48, C_Z = 64, C_U = 80, C_CS = 96, C_TMO = 127;
constexpr unsigned SPIN_CAP = 1u << 20;

struct Prm { const u16 *x, *wqkv, *wphi, *wo, *w_in, *w_sp, *b_sp, *w_out, *c, *w_mod; u16* out; char* ws; };

template <int N> DI void wait_vm() { asm volatile("s_waitcnt vmcnt(%0)" ::"n"(N) : "memory"); }
DI void wait_lgkm0() { __builtin_amdgcn_s_waitcnt(0xC07F); }
DI void bar() { asm volatile("" ::: "memory"); __builtin_amdgcn_s_barrier(); asm volatile("" ::: "memory"); }
template <int AUX> DI void glds16(const char* g, LAS char* l) { __builtin_amdgcn_global_load_lds((const GAS void*)g, (LAS void*)l, 16, 0, AUX); }
DI unsigned pk2(float a, float b) { f32x2 v = {a, b}; return __builtin_bit_cast(unsigned, __builtin_convertvector(v, bf16x2)); }
DI float bf_lo(unsigned u) { return __uint_as_float(u << 16); }
DI float bf_hi(unsigned u) { return __uint_as_float(u & 0xffff0000u); }
DI float rbf(float a) { return bf_lo(pk2(a, a)); }
DI bf16x8 pack8(const f32x4& a, const f32x4& b) { u32x4 r = {pk2(a[0], a[1]), pk2(a[2], a[3]), pk2(b[0], b[1]), pk2(b[2], b[3])}; return __builtin_bit_cast(bf16x8, r); }
DI f32x4 mfma16(bf16x8 a, bf16x8 b, f32x4 c) { return __builtin_amdgcn_mfma_f32_16x16x32_bf16(a, b, c, 0, 0, 0); }
DI s16x4 ldtr(unsigned a) { return __builtin_amdgcn_ds_read_tr16_b64_v4i16((LAS s16x4*)(size_t)a); }
DI bf16x8 cat8(s16x4 lo, s16x4 hi) { return __builtin_bit_cast(bf16x8, __builtin_shufflevector(lo, hi, 0, 1, 2, 3, 4, 5, 6, 7)); }
template <int CTRL> DI float dpp(float x) { return __builtin_bit_cast(float, __builtin_amdgcn_mov_dpp(__builtin_bit_cast(int, x), CTRL, 0xf, 0xf, true)); }
DI float x16max(float x) { auto s = __builtin_amdgcn_permlane16_swap(__float_as_uint(x), __float_as_uint(x), false, false); x = fmaxf(__uint_as_float(s[0]), __uint_as_float(s[1]));
  auto t = __builtin_amdgcn_permlane32_swap(__float_as_uint(x), __float_as_uint(x), false, false); return fmaxf(__uint_as_float(t[0]), __uint_as_float(t[1])); }
DI float x16min(float x) { auto s = __builtin_amdgcn_permlane16_swap(__float_as_uint(x), __float_as_uint(x), false, false); x = fminf(__uint_as_float(s[0]), __uint_as_float(s[1]));
  auto t = __builtin_amdgcn_permlane32_swap(__float_as_uint(x), __float_as_uint(x), false, false); return fminf(__uint_as_float(t[0]), __uint_as_float(t[1])); }
DI float x16sum(float x) { auto s = __builtin_amdgcn_permlane16_swap(__float_as_uint(x), __float_as_uint(x), false, false); x = __uint_as_float(s[0]) + __uint_as_float(s[1]);
  auto t = __builtin_amdgcn_permlane32_swap(__float_as_uint(x), __float_as_uint(x), false, false); return __uint_as_float(t[0]) + __uint_as_float(t[1]); }
DI float sum8(float x) { x += dpp<0xB1>(x); x += dpp<0x4E>(x); x += dpp<0x141>(x); return x; }
DI float sum64(float x) { x = sum8(x); x += dpp<0x128>(x); return x16sum(x); }
DI __amdgpu_buffer_rsrc_t mkrs(const void* p, unsigned bytes) { return __builtin_amdgcn_make_buffer_rsrc((void*)p, 0, bytes, 0x00020000); }
DI void st16_dev(__amdgpu_buffer_rsrc_t r, unsigned off, u32x4 v) { __builtin_amdgcn_raw_buffer_store_b128(v, r, off, 0, 16); }
DI void st8_dev(__amdgpu_buffer_rsrc_t r, unsigned off, u32x2 v) { __builtin_amdgcn_raw_buffer_store_b64(v, r, off, 0, 16); }
DI u32x4 ld16_dev(__amdgpu_buffer_rsrc_t r, unsigned off) { return __builtin_amdgcn_raw_buffer_load_b128(r, off, 0, 16); }
DI unsigned ld4_dev(__amdgpu_buffer_rsrc_t r, unsigned off) { return __builtin_amdgcn_raw_buffer_load_b32(r, off, 0, 16); }
DI void cnt_add(char* ws, int line) { __hip_atomic_fetch_add((GAS unsigned*)(ws + WS_CNT + line * 256), 1u, RLX_AGENT); }
DI void cnt_wait(char* ws, int line, unsigned want) {
  GAS unsigned* w = (GAS unsigned*)(ws + WS_CNT + line * 256); GAS unsigned* tmo = (GAS unsigned*)(ws + WS_CNT + C_TMO * 256);
  for (unsigned spins = 0;;) {
    if ((unsigned)__builtin_amdgcn_readfirstlane(__hip_atomic_load(w, RLX_AGENT)) >= want) break;
    __builtin_amdgcn_s_sleep(1);
    if (++spins > SPIN_CAP) { __hip_atomic_store(tmo, 1u, RLX_AGENT); break; }
    if ((spins & 255u) == 0u && __builtin_amdgcn_readfirstlane(__hip_atomic_load(tmo, RLX_AGENT))) break;
  }
  asm volatile("" ::: "memory");
}
DI float gelu_t(float x) {
  const float k1 = -2.0f * 0.7978845608028654f * 1.4426950408889634f, k3 = k1 * 0.044715f;
  const float e = __builtin_amdgcn_exp2f(x * __builtin_fmaf(x * x, k3, k1));
  return x * __builtin_amdgcn_rcpf(1.0f + e);
}

__global__ __launch_bounds__(256) void k_mod(Prm p) {
  __shared__ float red[4][2][64];
  const int tid = threadIdx.x, lane = tid & 63, wave = tid >> 6, b = blockIdx.x;
  if (tid < 32) ((unsigned*)(p.ws + WS_CNT))[b * 32 + tid] = 0u;
  const int rr = tid >> 3, ch = tid & 7;
  const int ca = (b & 31) * 64, ka = (b >> 5) * 128, cbb = 2048 + (b & 63) * 64, kb = (b >> 6) * 256;
  u32x4 wa[4], wb[8]; u16 c_a[4], c_b[8];
  UNR for (int i = 0; i < 4; ++i) { wa[i] = *(const u32x4*)(p.w_mod + (size_t)(ka + rr + 32 * i) * 6144 + ca + ch * 8); c_a[i] = p.c[ka + rr + 32 * i]; }
  UNR for (int i = 0; i < 8; ++i) { wb[i] = *(const u32x4*)(p.w_mod + (size_t)(kb + rr + 32 * i) * 6144 + cbb + ch * 8); c_b[i] = p.c[kb + rr + 32 * i]; }
  float a[8], bb[8];
  UNR for (int j = 0; j < 8; ++j) { a[j] = 0.f; bb[j] = 0.f; }
  UNR for (int i = 0; i < 4; ++i) { const float cv = bf_lo(c_a[i]); const float s = rbf(cv / (1.0f + __expf(-cv)));
    UNR for (int j = 0; j < 4; ++j) { a[2 * j] = __builtin_fmaf(s, bf_lo(wa[i][j]), a[2 * j]); a[2 * j + 1] = __builtin_fmaf(s, bf_hi(wa[i][j]), a[2 * j + 1]); } }
  UNR for (int i = 0; i < 8; ++i) { const float cv = bf_lo(c_b[i]); const float s = rbf(cv / (1.0f + __expf(-cv)));
    UNR for (int j = 0; j < 4; ++j) { bb[2 * j] = __builtin_fmaf(s, bf_lo(wb[i][j]), bb[2 * j]); bb[2 * j + 1] = __builtin_fmaf(s, bf_hi(wb[i][j]), bb[2 * j + 1]); } }
  UNR for (int j = 0; j < 8; ++j) { a[j] += dpp<0x128>(a[j]); a[j] = x16sum(a[j]); bb[j] += dpp<0x128>(bb[j]); bb[j] = x16sum(bb[j]); }
  if (lane < 8) { UNR for (int j = 0; j < 8; ++j) { red[wave][0][lane * 8 + j] = a[j]; red[wave][1][lane * 8 + j] = bb[j]; } }
  __syncthreads();
  if (tid < 128) { const int w = tid >> 6, c = tid & 63; const float v = (red[0][w][c] + red[1][w][c]) + (red[2][w][c] + red[3][w][c]);
    if (w == 0) ((float*)(p.ws + WS_MODA))[(b >> 5) * 2048 + ca + c] = v; else ((float*)(p.ws + WS_MODB))[(b >> 6) * 4096 + (cbb - 2048) + c] = v; }
}

__global__ __launch_bounds__(256) void k_h(Prm p) {
  __shared__ __attribute__((aligned(16))) float tab[2048];
  const int tid = threadIdx.x, lane = tid & 63, wave = tid >> 6, b = blockIdx.x;
  u32x4 xv[2][2];
  UNR for (int r = 0; r < 2; ++r) UNR for (int i = 0; i < 2; ++i) xv[r][i] = *(const u32x4*)(p.x + (size_t)(b * 8 + wave * 2 + r) * D_ + i * 512 + lane * 8);
  { const f32x4* pa = (const f32x4*)(p.ws + WS_MODA);
    UNR for (int i = 0; i < 2; ++i) { f32x4 s = pa[tid + 256 * i]; UNR for (int k = 1; k < 8; ++k) s += pa[k * 512 + tid + 256 * i];
      UNR for (int j = 0; j < 4; ++j) { s[j] = rbf(s[j]); if (i == 1) s[j] = rbf(1.0f + s[j]); }
      *(f32x4*)(tab + (tid + 256 * i) * 4) = s; } }
  if (b < 4) { const f32x4* pb = (const f32x4*)(p.ws + WS_MODB); const int i = b * 256 + tid; f32x4 s = (pb[i] + pb[1024 + i]) + (pb[2048 + i] + pb[3072 + i]);
    UNR for (int j = 0; j < 4; ++j) { s[j] = rbf(s[j]); if (b == 2) s[j] = rbf(1.0f + s[j]); }
    ((f32x4*)(p.ws + WS_MODF))[i] = s; }
  __syncthreads();
  UNR for (int r = 0; r < 2; ++r) {
    float v[16]; UNR for (int i = 0; i < 2; ++i) UNR for (int j = 0; j < 4; ++j) { v[i * 8 + 2 * j] = bf_lo(xv[r][i][j]); v[i * 8 + 2 * j + 1] = bf_hi(xv[r][i][j]); }
    float s = 0.f; UNR for (int j = 0; j < 16; ++j) s += v[j];
    const float mu = sum64(s) * (1.0f / 1024.0f); float q = 0.f;
    UNR for (int j = 0; j < 16; ++j) { v[j] -= mu; q = __builtin_fmaf(v[j], v[j], q); }
    const float rs = __builtin_amdgcn_rsqf(sum64(q) * (1.0f / 1024.0f) + 1e-5f);
    UNR for (int i = 0; i < 2; ++i) { const int c0 = i * 512 + lane * 8; float y[8];
      const f32x4 s0 = *(const f32x4*)(tab + c0), s1 = *(const f32x4*)(tab + c0 + 4), c0v = *(const f32x4*)(tab + 1024 + c0), c1v = *(const f32x4*)(tab + 1024 + c0 + 4);
      UNR for (int j = 0; j < 4; ++j) { y[j] = __builtin_fmaf(v[i * 8 + j] * rs, c0v[j], s0[j]); y[4 + j] = __builtin_fmaf(v[i * 8 + 4 + j] * rs, c1v[j], s1[j]); }
      u32x4 o = {pk2(y[0], y[1]), pk2(y[2], y[3]), pk2(y[4], y[5]), pk2(y[6], y[7])};
      *(u32x4*)((u16*)(p.ws + WS_H) + (size_t)(b * 8 + wave * 2 + r) * D_ + c0) = o; }
  }
}

struct LdAddr { unsigned a, b; };
DI unsigned voff_a(int lw, int lane, int lda) { const int r = 8 * lw + (lane >> 3); return (unsigned)(r * lda * 2 + (((lane & 7) ^ ((r >> 1) & 7)) * 16)); }
DI unsigned voff_b(int lw, int lane, int ldb) { const int rip = lane >> 3, f = ((rip >> 1) & 1) | ((lw & 1) << 1); return (unsigned)((8 * lw + rip) * ldb * 2 + (((lane & 7) ^ (f << 1)) * 16)); }
template <int NP, int AUXA, int AUXB>
DI void issue_tile(LAS char* s, const char* ga, int lda, unsigned va, const char* gb, int ldb, int pstride, unsigned vb) {
  UNR for (int j = 0; j < 4; ++j) glds16<AUXA>(ga + (size_t)j * 32 * lda * 2 + va, s + j * 4096);
  UNR for (int j = 0; j < 2 * NP; ++j) glds16<AUXB>(gb + (size_t)(j & 1) * 32 * ldb * 2 + (size_t)(j >> 1) * pstride * 2 + vb, s + 16384 + j * 4096);
}
template <int NST, int P, int KT, int TAIL, class F>
DI void loader_run(F issue) {
  issue(0, 0); issue(1, 1);
  wait_vm<P>(); bar();
  UNR for (int t = 2; t < NST; ++t) issue(t, t);
  int slot = 0;
  for (int t = 0; t < KT - NST; ++t) { wait_vm<(NST - 2) * P>(); bar(); issue(t + NST, slot); slot = (slot + 1 == NST) ? 0 : slot + 1; }
  UNR for (int i = 0; i < TAIL; ++i) { const int al = NST - 2 - i;
    if (al >= 4) wait_vm<4 * P>(); else if (al == 3) wait_vm<3 * P>(); else if (al == 2) wait_vm<2 * P>(); else if (al == 1) wait_vm<P>(); else wait_vm<0>();
    bar(); }
}

template <int NP, int KS, int NST, int NSWAP>
struct Cons {
  static constexpr int MT = 4, NT = 2 * NP, STG = 16384 + NP * 8192, NR1 = MT + 2 * NT, NM1 = MT * NT, NR = NR1 * KS, NM = NM1 * KS, NADV = 4;
  static constexpr int RPS = (NR + NADV <= NM) ? 1 : 2;
  f32x4 acc[MT][NT]; bf16x8 fa[2][KS][MT]; s16x4 fb[2][KS][NT][2]; unsigned ad[4]; int slot;
  DI void init(unsigned sb, int wm, int wn, int lane) {
    const int r = lane & 15, g = lane >> 4, q = r >> 2, p4 = lane & 3, f = ((q >> 1) & 1) | ((g & 1) << 1), arow = wm * 64 + r;
    ad[0] = sb + arow * 128 + ((g ^ ((r >> 1) & 7)) * 16); ad[1] = sb + arow * 128 + (((4 + g) ^ ((r >> 1) & 7)) * 16);
    ad[2] = sb + 16384 + (8 * g + q) * 128 + (((2 * wn) ^ f) * 32) + p4 * 8; ad[3] = sb + 16384 + (8 * g + q) * 128 + (((2 * wn + 1) ^ f) * 32) + p4 * 8;
    slot = 0; zero();
  }
  DI void zero() { UNR for (int i = 0; i < MT; ++i) UNR for (int j = 0; j < NT; ++j) acc[i][j] = (f32x4){0.f, 0.f, 0.f, 0.f}; }
  DI void rd1(int buf, int kb, int ks, int n) {
    if (n == 0) { fa[buf][kb][0] = *(LAS bf16x8*)(size_t)(ad[ks]); return; }
    if (n > 2 * NT) { const int i = n - 2 * NT; fa[buf][kb][i] = *(LAS bf16x8*)(size_t)(ad[ks] + i * 2048); return; }
    const int j = (n - 1) >> 1, hf = (n - 1) & 1;
    fb[buf][kb][j][hf] = ldtr(ad[2 + (j & 1)] + (j >> 1) * 8192 + ks * 4096 + hf * 512);
  }
  DI void mm1(int buf, int kb, int n) {
    const int i = n / NT, j = n % NT; const bf16x8 b = cat8(fb[buf][kb][j][0], fb[buf][kb][j][1]);
    acc[i][j] = (j >= NSWAP) ? mfma16(fa[buf][kb][i], b, acc[i][j]) : mfma16(b, fa[buf][kb][i], acc[i][j]);
  }
  DI int next_delta() { const bool w = (slot + 1 == NST); slot = w ? 0 : slot + 1; return w ? -(NST - 1) * STG : STG; }
  DI void phase(int use, int ks0, bool rd, bool adv, int delta) {
    UNR for (int n = 0; n < NM; ++n) {
      mm1(use, n / NM1, n % NM1); SB();
      UNR for (int u = 0; u < RPS; ++u) { const int x = n * RPS + u;
        if (rd && x < NR) rd1(use ^ 1, x / NR1, ks0 + x / NR1, x % NR1);
        if (adv && x >= NR && x < NR + NADV) { ad[x - NR] += delta; asm volatile("" : "+v"(ad[x - NR])); } }
      SB();
    }
  }
  DI void start() {
    bar();
    UNR for (int n = 0; n < NR; ++n) rd1(0, n / NR1, n / NR1, n % NR1);
    if (KS == 2) { const int d = next_delta(); UNR for (int n = 0; n < NADV; ++n) ad[n] += d; }
    wait_lgkm0(); SB();
  }
  DI void run_half(int kt) {
    for (int t = 0; t < kt - 1; ++t) { const int d = next_delta(); phase(0, 1, true, true, d); wait_lgkm0(); bar(); SB(); phase(1, 0, true, false, 0); wait_lgkm0(); SB(); }
    phase(0, 1, true, false, 0); wait_lgkm0(); bar(); SB(); phase(1, 0, false, false, 0);
  }
  DI void run_pairs(int n) {
    for (int t = 0; t < n; t += 2) {
      bar(); SB(); { const int d = next_delta(); phase(0, 0, true, true, d); } wait_lgkm0();
      bar(); SB(); { const int d = next_delta(); phase(1, 0, true, true, d); } wait_lgkm0(); SB();
    }
  }
  DI void run_last2() { bar(); SB(); phase(0, 0, true, false, 0); wait_lgkm0(); SB(); phase(1, 0, false, false, 0); }
};

template <int NP2> DI f32x2 row_stats(const char* base, int row) {
  const __amdgpu_buffer_rsrc_t rs = mkrs(base, NP2 * 2048 * 16);
  u32x4 v[NP2]; UNR for (int i = 0; i < NP2; ++i) v[i] = ld16_dev(rs, (unsigned)(i * 2048 + row) * 16);
  float m = 0.f; UNR for (int i = 0; i < NP2; ++i) m += __uint_as_float(v[i][0]) + __uint_as_float(v[i][2]);
  m *= 1.0f / (2 * NP2); float q = 0.f;
  UNR for (int i = 0; i < NP2; ++i) { const float d0 = __uint_as_float(v[i][0]) - m, d1 = __uint_as_float(v[i][2]) - m; q += (__uint_as_float(v[i][1]) + __uint_as_float(v[i][3])) + 64.0f * (d0 * d0 + d1 * d1); }
  return (f32x2){m, __builtin_amdgcn_rsqf(q * (1.0f / (128 * NP2)) + 1e-5f)};
}

DI void softmax_pm(const f32x4& p0, const f32x4& p1, f32x4& a0, f32x4& a1, f32x4& b0, f32x4& b1) {
  const float L2E = 1.4426950408889634f;
  float mx = fmaxf(fmaxf(fmaxf(p0[0], p0[1]), fmaxf(p0[2], p0[3])), fmaxf(fmaxf(p1[0], p1[1]), fmaxf(p1[2], p1[3])));
  float mn = fminf(fminf(fminf(p0[0], p0[1]), fminf(p0[2], p0[3])), fminf(fminf(p1[0], p1[1]), fminf(p1[2], p1[3])));
  mx = x16max(mx) * L2E; mn = x16min(mn) * L2E;
  UNR for (int e = 0; e < 4; ++e) { a0[e] = __builtin_amdgcn_exp2f(__builtin_fmaf(p0[e], L2E, -mx)); a1[e] = __builtin_amdgcn_exp2f(__builtin_fmaf(p1[e], L2E, -mx));
                                    b0[e] = __builtin_amdgcn_exp2f(__builtin_fmaf(p0[e], -L2E, mn)); b1[e] = __builtin_amdgcn_exp2f(__builtin_fmaf(p1[e], -L2E, mn)); }
  const float ra = __builtin_amdgcn_rcpf(x16sum(((a0[0] + a0[1]) + (a0[2] + a0[3])) + ((a1[0] + a1[1]) + (a1[2] + a1[3]))));
  const float rb = __builtin_amdgcn_rcpf(x16sum(((b0[0] + b0[1]) + (b0[2] + b0[3])) + ((b1[0] + b1[1]) + (b1[2] + b1[3]))));
  a0 *= ra; a1 *= ra; b0 *= rb; b1 *= rb;
}
template <bool PUB>
DI void phase_qkv(const Prm& p, LAS char* smem, int wave, int lane, int tid) {
  constexpr int NST = 3, KT = 16, L_PHI = 0, L_XKV = 34816, L_KVS = 59392, L_ZS = 67584, L_OST = 68096, L_WPHI = 122880;
  const int xcd = blockIdx.x & 7, idx = blockIdx.x >> 3, rb = (xcd >> 2) * 8 + (idx >> 2), hp = (xcd & 3) * 4 + (idx & 3);
  const unsigned sb = (unsigned)(size_t)smem; const int r = lane & 15, g = lane >> 4, wm = (wave >> 1) & 1, wn = wave & 1;
  bf16x8 pf[4][2];
  if (wave >= 4) {
    const int lw = wave - 4; const char* A = p.ws + WS_H + (size_t)rb * 128 * D_ * 2; const char* B = (const char*)p.wqkv + hp * 128;
    const unsigned va = voff_a(lw, lane, D_), vb = voff_b(lw, lane, 3072);
    glds16<0>((const char*)p.wphi + hp * 4096 + lw * 1024 + lane * 16, smem + L_WPHI + lw * 1024);
    loader_run<NST, 10, KT, NST>([&](int t, int slot) { issue_tile<3, 0, 0>(smem + slot * 40960 + lw * 1024, A + t * 128, D_, va, B + (size_t)t * 64 * 3072 * 2, 3072, 1024, vb); });
    bar();
  } else {
    Cons<3, 1, NST, 4> c; c.init(sb, wm, wn, lane); c.start(); c.run_half(KT);
    const int q = r >> 2, p4 = lane & 3;
    bf16x8 wf[2];
    UNR for (int ft = 0; ft < 2; ++ft) { const unsigned a = sb + L_WPHI + wn * 2048 + (4 * g + q) * 64 + ft * 32 + p4 * 8; wf[ft] = cat8(ldtr(a), ldtr(a + 1024)); }
    const f32x4 z4 = {0.f, 0.f, 0.f, 0.f};
    LAS char* phi = smem + L_PHI + wave * (64 * 136);
    UNR for (int mt = 0; mt < 4; ++mt) {
      const bf16x8 kf = pack8(c.acc[mt][2], c.acc[mt][3]); f32x4 t[4];
      softmax_pm(mfma16(wf[0], kf, z4), mfma16(wf[1], kf, z4), t[0], t[1], t[2], t[3]);
      UNR for (int ft = 0; ft < 4; ++ft) *(LAS u32x2*)(phi + (16 * mt + r) * 136 + (16 * ft + 4 * g) * 2) = (u32x2){pk2(t[ft][0], t[ft][1]), pk2(t[ft][2], t[ft][3])};
    }
    f32x4 kv[4][2], zz[4];
    { const u32x4 o4 = {0x3f803f80u, 0x3f803f80u, 0x3f803f80u, 0x3f803f80u}; const bf16x8 ones = __builtin_bit_cast(bf16x8, o4);
      UNR for (int ft = 0; ft < 4; ++ft) { kv[ft][0] = z4; kv[ft][1] = z4; zz[ft] = z4; }
      UNR for (int kk = 0; kk < 2; ++kk) { const bf16x8 v0 = pack8(c.acc[2 * kk][4], c.acc[2 * kk + 1][4]), v1 = pack8(c.acc[2 * kk][5], c.acc[2 * kk + 1][5]);
        UNR for (int ft = 0; ft < 4; ++ft) { const unsigned a = (unsigned)(size_t)phi + (32 * kk + 4 * g + q) * 136 + ft * 32 + p4 * 8; const bf16x8 pk = cat8(ldtr(a), ldtr(a + 16 * 136));
          kv[ft][0] = mfma16(pk, v0, kv[ft][0]); kv[ft][1] = mfma16(pk, v1, kv[ft][1]); zz[ft] = mfma16(pk, ones, zz[ft]); } } }
    LAS char* xk = smem + L_XKV + wn * 12288 + lane * 16;
    if (wm == 1) { UNR for (int ft = 0; ft < 4; ++ft) { *(LAS f32x4*)(xk + (3 * ft) * 1024) = kv[ft][0]; *(LAS f32x4*)(xk + (3 * ft + 1) * 1024) = kv[ft][1]; *(LAS f32x4*)(xk + (3 * ft + 2) * 1024) = zz[ft]; } wait_lgkm0(); }
    bar();
    if (wm == 0) {
      UNR for (int ft = 0; ft < 4; ++ft) { kv[ft][0] += *(LAS f32x4*)(xk + (3 * ft) * 1024); kv[ft][1] += *(LAS f32x4*)(xk + (3 * ft + 1) * 1024); zz[ft] += *(LAS f32x4*)(xk + (3 * ft + 2) * 1024); }
      const int slotid = (2 * hp + wn) * 16 + rb;
      const __amdgpu_buffer_rsrc_t rk = mkrs(p.ws + WS_KVP, 2u << 20), rz = mkrs(p.ws + WS_ZP, 131072);
      UNR for (int dt = 0; dt < 2; ++dt) UNR for (int k2 = 0; k2 < 2; ++k2) st16_dev(rk, (unsigned)(slotid * 4 + dt * 2 + k2) * 1024 + lane * 16, __builtin_bit_cast(u32x4, pack8(kv[2 * k2][dt], kv[2 * k2 + 1][dt])));
      const f32x4 zs = r == 0 ? zz[0] : r == 1 ? zz[1] : r == 2 ? zz[2] : zz[3];
      if (r < 4) st16_dev(rz, (unsigned)(slotid * 64 + 16 * r + 4 * g) * 4, __builtin_bit_cast(u32x4, zs));
      wait_vm<0>();
      if (lane == 0) cnt_add(p.ws, C_KV + hp);
    }
    UNR for (int mt = 0; mt < 4; ++mt) {
      const bf16x8 qf = pack8(c.acc[mt][0], c.acc[mt][1]); f32x4 t[4];
      softmax_pm(mfma16(wf[0], qf, z4), mfma16(wf[1], qf, z4), t[0], t[1], t[2], t[3]);
      pf[mt][0] = pack8(t[0], t[1]); pf[mt][1] = pack8(t[2], t[3]);
    }
  }
  {
    cnt_wait(p.ws, C_KV + hp, 32u);
    const int hl = wave >> 2, ck = wave & 3, head = 2 * hp + hl;
    const __amdgpu_buffer_rsrc_t rk = mkrs(p.ws + WS_KVP, 2u << 20), rz = mkrs(p.ws + WS_ZP, 131072);
    u32x4 v[16]; UNR for (int i = 0; i < 16; ++i) v[i] = ld16_dev(rk, (unsigned)((head * 16 + i) * 4 + ck) * 1024 + lane * 16);
    if (ck == 0) { float zs = 0.f; unsigned zv[16]; UNR for (int i = 0; i < 16; ++i) zv[i] = ld4_dev(rz, (unsigned)((head * 16 + i) * 64 + lane) * 4);
      UNR for (int i = 0; i < 16; ++i) zs += __uint_as_float(zv[i]);
      *(LAS u16*)(smem + L_ZS + hl * 128 + lane * 2) = (u16)pk2(zs, zs); }
    f32x4 lo = {0.f, 0.f, 0.f, 0.f}, hi = lo;
    UNR for (int i = 0; i < 16; ++i) UNR for (int j = 0; j < 4; ++j) { lo[j] += bf_lo(v[i][j]); hi[j] += bf_hi(v[i][j]); }
    *(LAS u32x4*)(smem + L_KVS + hl * 4096 + ck * 1024 + lane * 16) = (u32x4){pk2(lo[0], hi[0]), pk2(lo[1], hi[1]), pk2(lo[2], hi[2]), pk2(lo[3], hi[3])};
    wait_lgkm0(); bar();
  }
  if (wave < 4) {
    bf16x8 kf[2][2], zf[2];
    UNR for (int dt = 0; dt < 2; ++dt) UNR for (int k2 = 0; k2 < 2; ++k2) kf[dt][k2] = *(LAS bf16x8*)(smem + L_KVS + wn * 4096 + (dt * 2 + k2) * 1024 + lane * 16);
    UNR for (int k2 = 0; k2 < 2; ++k2) { LAS char* a = smem + L_ZS + wn * 128 + (32 * k2 + 4 * g) * 2; zf[k2] = cat8(*(LAS s16x4*)a, *(LAS s16x4*)(a + 32)); }
    const f32x4 z4 = {0.f, 0.f, 0.f, 0.f};
    UNR for (int mt = 0; mt < 4; ++mt) {
      const f32x4 den = mfma16(zf[1], pf[mt][1], mfma16(zf[0], pf[mt][0], z4)); const float rd = __builtin_amdgcn_rcpf(den[0] + 1e-6f);
      UNR for (int dt = 0; dt < 2; ++dt) { const f32x4 nu = mfma16(kf[dt][1], pf[mt][1], mfma16(kf[dt][0], pf[mt][0], z4)) * rd;
        *(LAS u32x2*)(smem + L_OST + (64 * wm + 16 * mt + r) * 144 + (32 * wn + 16 * dt + 4 * g) * 2) = (u32x2){pk2(nu[0], nu[1]), pk2(nu[2], nu[3])}; }
    }
    wait_lgkm0();
  }
  bar();
  { const __amdgpu_buffer_rsrc_t ro = mkrs(p.ws + WS_O, 4u << 20);
    UNR for (int i = 0; i < 2; ++i) { const int row = (tid >> 3) + 64 * i, ch = tid & 7;
      st16_dev(ro, (unsigned)((rb * 128 + row) * D_ + hp * 64 + ch * 8) * 2, *(LAS u32x4*)(smem + L_OST + row * 144 + ch * 16)); } }
  if (PUB) { wait_vm<0>(); bar(); if (tid == 0) cnt_add(p.ws, C_O + rb); }
}

template <bool LAST, bool WAIT, bool PUB>
DI void phase_proj(const Prm& p, LAS char* smem, int wave, int lane, int tid) {
  constexpr int NST = 6, KT = LAST ? 28 : 16, LDA = LAST ? FH : D_;
  const int xcd = blockIdx.x & 7, idx = blockIdx.x >> 3, rb = (xcd >> 1) * 4 + (idx >> 3), cb = (xcd & 1) * 8 + (idx & 7);
  const unsigned sb = (unsigned)(size_t)smem; const int r = lane & 15, g = lane >> 4, wm = (wave >> 1) & 1, wn = wave & 1;
  const int ch = tid & 7, col = cb * 64 + ch * 8; const u16* res = LAST ? (const u16*)(p.ws + WS_X1) : p.x; const float* mf = (const float*)(p.ws + WS_MODF);
  u32x4 xv[2]; f32x4 gv[2], cv[2];
  auto pre = [&]() {
    UNR for (int i = 0; i < 2; ++i) { xv[i] = *(const u32x4*)(res + (size_t)(rb * 128 + (tid >> 3) + 64 * i) * D_ + col); gv[i] = *(const f32x4*)(mf + (LAST ? 3072 : 0) + col + 4 * i); if (!LAST) cv[i] = *(const f32x4*)(mf + 2048 + col + 4 * i); }
    UNR for (int i = 0; i < 2; ++i) { asm volatile("" : "+v"(xv[i])); asm volatile("" : "+v"(gv[i])); if (!LAST) asm volatile("" : "+v"(cv[i])); } };
  if (wave >= 4) {
    const int lw = wave - 4; const char* A = p.ws + (LAST ? WS_U : WS_O) + (size_t)rb * 128 * LDA * 2; const char* B = (const char*)(LAST ? p.w_out : p.wo) + cb * 128;
    const unsigned va = voff_a(lw, lane, LDA), vb = voff_b(lw, lane, D_);
    if (WAIT) cnt_wait(p.ws, (LAST ? C_U : C_O) + rb, LAST ? 56u : 16u);
    loader_run<NST, 6, KT, NST - 1>([&](int t, int slot) { issue_tile<1, 16, 0>(smem + slot * 24576 + lw * 1024, A + t * 128, LDA, va, B + (size_t)t * 64 * D_ * 2, D_, 64, vb); });
    pre();
    bar();
  } else {
    pre();
    Cons<1, 2, NST, 2> c; c.init(sb, wm, wn, lane); c.start(); c.run_pairs(KT - 2); c.run_last2();
    bar();
    UNR for (int mt = 0; mt < 4; ++mt) UNR for (int j = 0; j < 2; ++j)
      *(LAS u32x2*)(smem + (64 * wm + 16 * mt + r) * 144 + (32 * wn + 16 * j + 4 * g) * 2) = (u32x2){pk2(c.acc[mt][j][0], c.acc[mt][j][1]), pk2(c.acc[mt][j][2], c.acc[mt][j][3])};
    wait_lgkm0();
  }
  bar();
  UNR for (int i = 0; i < 2; ++i) {
    const int row = (tid >> 3) + 64 * i; const u32x4 av = *(LAS u32x4*)(smem + row * 144 + ch * 16); float v[8];
    UNR for (int j = 0; j < 4; ++j) { v[2 * j] = bf_lo(xv[i][j]) + rbf(gv[j >> 1][(2 * j) & 3] * bf_lo(av[j])); v[2 * j + 1] = bf_hi(xv[i][j]) + rbf(gv[j >> 1][(2 * j + 1) & 3] * bf_hi(av[j])); }
    const u32x4 o = {pk2(v[0], v[1]), pk2(v[2], v[3]), pk2(v[4], v[5]), pk2(v[6], v[7])}; const size_t off = (size_t)(rb * 128 + row) * D_ + col;
    if (LAST) *(u32x4*)(p.out + off) = o;
    else {
      *(u32x4*)((u16*)(p.ws + WS_X1) + off) = o;
      UNR for (int j = 0; j < 4; ++j) { v[2 * j] = bf_lo(o[j]); v[2 * j + 1] = bf_hi(o[j]); }
      const u32x4 y = {pk2(v[0] * cv[0][0], v[1] * cv[0][1]), pk2(v[2] * cv[0][2], v[3] * cv[0][3]), pk2(v[4] * cv[1][0], v[5] * cv[1][1]), pk2(v[6] * cv[1][2], v[7] * cv[1][3])};
      st16_dev(mkrs(p.ws + WS_Y, 4u << 20), (unsigned)off * 2, y);
      const float mu = sum8(((v[0] + v[1]) + (v[2] + v[3])) + ((v[4] + v[5]) + (v[6] + v[7]))) * (1.0f / 64.0f); float q = 0.f;
      UNR for (int j = 0; j < 8; ++j) { const float d = v[j] - mu; q = __builtin_fmaf(d, d, q); }
      q = sum8(q);
      if (ch == 0) st8_dev(mkrs(p.ws + WS_ST1, 262144), (unsigned)(((cb >> 1) * 2048 + rb * 128 + row) * 16 + (cb & 1) * 8), (u32x2){__float_as_uint(mu), __float_as_uint(q)});
    }
  }
  if (PUB) { wait_vm<0>(); bar(); if (tid == 0) cnt_add(p.ws, C_Y + rb); }
}

DI void helper_cs(const Prm& p, LAS char* smem, int j, int tid) {
  const int ch = tid & 15, rr = tid >> 4; const float* mf = (const float*)(p.ws + WS_MODF); float a[8], b[8];
  UNR for (int e = 0; e < 8; ++e) { a[e] = 0.f; b[e] = 0.f; }
  for (int k0 = 0; k0 < 1024; k0 += 256) { u32x4 w[8]; float cc[8], ss[8];
    UNR for (int i = 0; i < 8; ++i) { const int k = k0 + rr + 32 * i; w[i] = *(const u32x4*)(p.w_in + (size_t)k * 3584 + j * 128 + ch * 8); cc[i] = mf[2048 + k]; ss[i] = mf[1024 + k]; }
    UNR for (int i = 0; i < 8; ++i) UNR for (int e = 0; e < 4; ++e) { const float lo = bf_lo(w[i][e]), hi = bf_hi(w[i][e]);
      a[2 * e] = __builtin_fmaf(cc[i], lo, a[2 * e]); a[2 * e + 1] = __builtin_fmaf(cc[i], hi, a[2 * e + 1]); b[2 * e] = __builtin_fmaf(ss[i], lo, b[2 * e]); b[2 * e + 1] = __builtin_fmaf(ss[i], hi, b[2 * e + 1]); } }
  UNR for (int e = 0; e < 8; ++e) { a[e] = x16sum(a[e]); b[e] = x16sum(b[e]); }
  LAS float* red = (LAS float*)smem; const int wave = tid >> 6;
  if ((tid & 63) < 16) { UNR for (int e = 0; e < 8; ++e) { red[wave * 256 + ch * 8 + e] = a[e]; red[wave * 256 + 128 + ch * 8 + e] = b[e]; } }
  wait_lgkm0(); bar();
  if (tid < 64) { f32x4 s = *(LAS f32x4*)(red + tid * 4); UNR for (int w = 1; w < 8; ++w) s += *(LAS f32x4*)(red + w * 256 + tid * 4);
    st16_dev(mkrs(p.ws + WS_CSB, 32768), (unsigned)((tid >> 5) * 3584 + j * 128 + (tid & 31) * 4) * 4, __builtin_bit_cast(u32x4, s));
    wait_vm<0>(); if (tid == 0) cnt_add(p.ws, C_CS + j); }
  bar();
}
template <bool WAIT, bool PUB>
DI void phase_gmlp(const Prm& p, LAS char* smem, int wave, int lane, int tid) {
  constexpr int NST = 4, L_STG = 131072;
  const int xcd = blockIdx.x & 7, idx = blockIdx.x >> 3;
  if (idx >= 28) { const int j = (idx - 28) * 8 + xcd; if (j < 28) helper_cs(p, smem, j, tid); return; }
  const int rb = (xcd >> 1) * 4 + idx / 7, cb = (xcd & 1) * 7 + idx % 7;
  if (wave >= 4) {
    const int lw = wave - 4; const char* A1 = p.ws + WS_Y + (size_t)rb * 128 * D_ * 2; const char* A2 = (const char*)p.w_sp + (size_t)rb * 128 * S_ * 2;
    const char* B1 = (const char*)p.w_in + cb * 256; const char* B2 = p.ws + WS_Z2N + cb * 256;
    const unsigned va1 = voff_a(lw, lane, D_), va2 = voff_a(lw, lane, S_), vb1 = voff_b(lw, lane, 3584), vb2 = voff_b(lw, lane, FH);
    if (WAIT) cnt_wait(p.ws, C_Y + rb, 16u);
    loader_run<NST, 8, 64, NST - 1>([&](int t, int slot) { LAS char* s = smem + slot * 32768 + lw * 1024;
      if (t < 32) issue_tile<2, 16, 0>(s, A1 + (t & 15) * 128, D_, va1, B1 + (size_t)(t & 15) * 64 * 3584 * 2 + (t < 16 ? FH * 2 : 0), 3584, 64, vb1);
      else { if (t == 32) cnt_wait(p.ws, C_Z + cb, 64u);
        issue_tile<2, 0, 16>(s, A2 + (t - 32) * 128, S_, va2, B2 + (size_t)(t - 32) * 64 * FH * 2, FH, 64, vb2); } });
  } else {
    const unsigned sb = (unsigned)(size_t)smem; const int r = lane & 15, g = lane >> 4, wm = wave >> 1, wn = wave & 1;
    Cons<2, 2, NST, 4> c; c.init(sb, wm, wn, lane);
    float mu1[4], rs1[4];
    LAS char* stg = smem + L_STG + wave * 8192;
    const int row0 = rb * 128 + wm * 64;
    auto frag_at = [&](int mt, int j) { const int row = 16 * mt + r; return (LAS u32x2*)(stg + row * 128 + (((j * 2 + (g >> 1)) ^ (row & 7)) * 16) + (g & 1) * 8); };
    auto pre_act = [&](int colbase, bool stats) {
      const __amdgpu_buffer_rsrc_t rc = mkrs(p.ws + WS_CSB, 32768); f32x4 cs[4], bi[4]; float ms = 0.f, qs = 0.f;
      UNR for (int j = 0; j < 4; ++j) { const unsigned o = (unsigned)(colbase + 64 * (j >> 1) + 32 * wn + 16 * (j & 1) + 4 * g) * 4;
        cs[j] = __builtin_bit_cast(f32x4, ld16_dev(rc, o)); bi[j] = __builtin_bit_cast(f32x4, ld16_dev(rc, o + 3584 * 4)); }
      UNR for (int mt = 0; mt < 4; ++mt) { u32x2 zp[4];
        UNR for (int j = 0; j < 4; ++j) { float z[4];
          UNR for (int e = 0; e < 4; ++e) z[e] = __builtin_fmaf(rs1[mt], __builtin_fmaf(-mu1[mt], cs[j][e], c.acc[mt][j][e]), bi[j][e]);
          const unsigned a = pk2(z[0], z[1]), b = pk2(z[2], z[3]);
          zp[j] = (u32x2){pk2(gelu_t(bf_lo(a)), gelu_t(bf_hi(a))), pk2(gelu_t(bf_lo(b)), gelu_t(bf_hi(b)))}; *frag_at(mt, j) = zp[j]; }
        if (stats) { float s = 0.f; UNR for (int j = 0; j < 4; ++j) s += (bf_lo(zp[j][0]) + bf_hi(zp[j][0])) + (bf_lo(zp[j][1]) + bf_hi(zp[j][1]));
          const float m = x16sum(s) * (1.0f / 64.0f); float q = 0.f;
          UNR for (int j = 0; j < 4; ++j) UNR for (int e = 0; e < 2; ++e) { const float d0 = bf_lo(zp[j][e]) - m, d1 = bf_hi(zp[j][e]) - m; q = __builtin_fmaf(d0, d0, __builtin_fmaf(d1, d1, q)); }
          q = x16sum(q); if (g == mt) { ms = m; qs = q; } }
        SB(); }
      if (stats) st8_dev(mkrs(p.ws + WS_ST2, 14 * 2048 * 16), (unsigned)((cb * 2048 + row0 + lane) * 16 + wn * 8), (u32x2){__float_as_uint(ms), __float_as_uint(qs)}); };
    auto store_tile = [&](size_t wsoff, bool norm, f32x2 st) {
      const __amdgpu_buffer_rsrc_t ro = mkrs(p.ws + wsoff, 8u << 20);
      UNR for (int i = 0; i < 8; ++i) { const int row = 8 * i + (lane >> 3), chn = lane & 7;
        u32x4 v = *(LAS u32x4*)(stg + row * 128 + ((chn ^ (row & 7)) * 16));
        if (norm) { const float rs = __shfl(st[1], row), nb = -__shfl(st[0], row) * rs;
          UNR for (int e = 0; e < 4; ++e) v[e] = pk2(__builtin_fmaf(bf_lo(v[e]), rs, nb), __builtin_fmaf(bf_hi(v[e]), rs, nb)); }
        st16_dev(ro, (unsigned)((row0 + row) * FH + cb * 128 + 64 * (chn >> 2) + 32 * wn + (chn & 3) * 8) * 2, v); } };
    c.start(); c.run_pairs(16);
    { if (WAIT) cnt_wait(p.ws, C_Y + rb, 16u);
      const f32x2 st = row_stats<8>(p.ws + WS_ST1, row0 + lane);
      UNR for (int mt = 0; mt < 4; ++mt) { mu1[mt] = __shfl(st[0], 16 * mt + r); rs1[mt] = __shfl(st[1], 16 * mt + r); }
      cnt_wait(p.ws, C_CS + 14 + cb, 1u);
      pre_act(FH + cb * 128, true);
      wait_vm<0>(); if (lane == 0) cnt_add(p.ws, C_S2 + rb);
      c.zero(); wait_lgkm0(); SB(); }
    c.run_pairs(8);
    { cnt_wait(p.ws, C_S2 + rb, 56u);
      store_tile(WS_Z2N, true, row_stats<14>(p.ws + WS_ST2, row0 + lane)); wait_lgkm0(); SB(); }
    c.run_pairs(2);
    wait_vm<0>(); if (lane == 0) cnt_add(p.ws, C_Z + cb);
    c.run_pairs(6);
    cnt_wait(p.ws, C_CS + cb, 1u);
    pre_act(cb * 128, false); c.zero(); wait_lgkm0(); SB();
    c.run_pairs(30); c.run_last2();
    UNR for (int mt = 0; mt < 4; ++mt) { const float bs = bf_lo(p.b_sp[row0 + 16 * mt + r]);
      UNR for (int j = 0; j < 4; ++j) { const unsigned a = pk2(c.acc[mt][j][0], c.acc[mt][j][1]), b = pk2(c.acc[mt][j][2], c.acc[mt][j][3]);
        const unsigned a2 = pk2(bf_lo(a) + bs, bf_hi(a) + bs), b2 = pk2(bf_lo(b) + bs, bf_hi(b) + bs); const u32x2 z1 = *frag_at(mt, j);
        *frag_at(mt, j) = (u32x2){pk2(bf_lo(z1[0]) * bf_lo(a2), bf_hi(z1[0]) * bf_hi(a2)), pk2(bf_lo(z1[1]) * bf_lo(b2), bf_hi(z1[1]) * bf_hi(b2))}; } }
    store_tile(WS_U, false, (f32x2){0.f, 0.f});
    if (PUB) { wait_vm<0>(); if (lane == 0) cnt_add(p.ws, C_U + rb); }
  }
  if (PUB) bar();
}

template <int LO, int HI>
__global__ __launch_bounds__(512, 2) void k_main(Prm p) {
  extern __shared__ __attribute__((aligned(16))) char smem_[];
  LAS char* smem = (LAS char*)smem_;
  const int tid = threadIdx.x, lane = tid & 63, wave = __builtin_amdgcn_readfirstlane(tid >> 6);
  if (LO <= 1 && HI >= 1) phase_qkv<(HI > 1)>(p, smem, wave, lane, tid);
  if (LO <= 2 && HI >= 2) phase_proj<false, (LO < 2), (HI > 2)>(p, smem, wave, lane, tid);
  if (LO <= 3 && HI >= 3) phase_gmlp<(LO < 3), (HI > 3)>(p, smem, wave, lane, tid);
  if (LO <= 4 && HI >= 4) phase_proj<true, (LO < 4), false>(p, smem, wave, lane, tid);
}

#define FUSE 1
template <int LO, int HI> static void launch_main(const Prm& p, hipStream_t stream) {
  (void)hipFuncSetAttribute((const void*)k_main<LO, HI>, hipFuncAttributeMaxDynamicSharedMemorySize, 163840);
  k_main<LO, HI><<<256, 512, 163840, stream>>>(p);
}
extern "C" void kernel_launch(void* const* d_in, const int* in_sizes, int n_in, void* d_out, int out_size, void* d_ws, size_t ws_size, hipStream_t stream) {
  Prm p;
  p.x = (const u16*)d_in[0]; p.wqkv = (const u16*)d_in[1]; p.wphi = (const u16*)d_in[2]; p.wo = (const u16*)d_in[3];
  p.w_in = (const u16*)d_in[5]; p.w_sp = (const u16*)d_in[6]; p.b_sp = (const u16*)d_in[7]; p.w_out = (const u16*)d_in[8];
  p.c = (const u16*)d_in[9]; p.w_mod = (const u16*)d_in[10]; p.out = (u16*)d_out; p.ws = (char*)d_ws;
  k_mod<<<256, 256, 0, stream>>>(p);
  k_h<<<256, 256, 0, stream>>>(p);
  launch_main<1, 4>(p, stream);
}
```

```cpp
#include <hip/hip_runtime.h>
#include <stdint.h>

typedef unsigned short u16;
typedef __bf16 bf16x8 __attribute__((ext_vector_type(8)));
typedef __bf16 bf16x2 __attribute__((ext_vector_type(2)));
typedef short s16x4 __attribute__((ext_vector_type(4)));
typedef float f32x2 __attribute__((ext_vector_type(2)));
typedef float f32x4 __attribute__((ext_vector_type(4)));
typedef unsigned u32x4 __attribute__((ext_vector_type(4)));
typedef unsigned u32x2 __attribute__((ext_vector_type(2)));
#define LAS __attribute__((address_space(3)))
#define GAS __attribute__((address_space(1)))
#define DI __device__ __forceinline__
#define SB() __builtin_amdgcn_sched_barrier(0)
#define RLX_AGENT __ATOMIC_RELAXED, __HIP_MEMORY_SCOPE_AGENT
#define UNR _Pragma("unroll")

constexpr int S_ = 2048, D_ = 1024, FH = 1792;
constexpr size_t WS_CNT  = 0;
constexpr size_t WS_MODA = 65536;
constexpr size_t WS_MODB = WS_MODA + 65536;
constexpr size_t WS_MODF = WS_MODB + 65536;
constexpr size_t WS_CSB  = WS_MODF + 16384;
constexpr size_t WS_ZP   = WS_CSB + 65536;
constexpr size_t WS_ST1  = WS_ZP + 131072;
constexpr size_t WS_ST2  = WS_ST1 + 262144;
constexpr size_t WS_KVP  = 1u << 20;
constexpr size_t WS_H    = WS_KVP + (2u << 20);
constexpr size_t WS_O    = WS_H + (4u << 20);
constexpr size_t WS_X1   = WS_O + (4u << 20);
constexpr size_t WS_Y    = WS_X1 + (4u << 20);
constexpr size_t WS_Z2N  = WS_Y + (4u << 20);
constexpr size_t WS_U    = WS_Z2N + (8u << 20);
constexpr size_t WS_END  = WS_U + (8u << 20);
static_assert(WS_ST2 + 14 * 2048 * 16 <= WS_KVP, "ws map");
constexpr int C_KV = 0, C_O = 16, C_Y = 32, C_S2 = 48, C_Z = 64, C_U = 80, C_CS = 96, C_TMO = 127, C_H = 128;
constexpr unsigned SPIN_CAP = 1u << 20;

#define STAMPS 0
constexpr size_t WS_STAMP = 64u << 20;
#define WS_TMO_OFF (WS_CNT + C_TMO * 256)
#define STAMP(k, i) do {} while (0)
#define STAMPL(k, i) do {} while (0)
#define STAMPC(i) do {} while (0)
#define STAMPS2(i) do {} while (0)
#define PF0 96
#define PACE 30
#define G1_ALLB 0
#define CODEPF 1
struct Prm { const u16 *x, *wqkv, *wphi, *wo, *w_in, *w_sp, *b_sp, *w_out, *c, *w_mod; u16* out; char* ws; };

template <int N> DI void wait_vm() { asm volatile("s_waitcnt vmcnt(%0)" ::"n"(N) : "memory"); }
DI void wait_lgkm0() { __builtin_amdgcn_s_waitcnt(0xC07F); }
DI void bar() { asm volatile("" ::: "memory"); __builtin_amdgcn_s_barrier(); asm volatile("" ::: "memory"); }
#define DMA_OP(sfx, n, off) "s_add_i32 m0, %1, " off "\n\ts_nop 0\n\tglobal_load_lds_dwordx4 %2, %" n sfx "\n\t"
#define DMA4_STR(sfx) "s_nop 4\n\ts_mov_b32 %0, m0\n\t" DMA_OP(sfx, "3", "0") DMA_OP(sfx, "4", "0x1000") DMA_OP(sfx, "5", "0x2000") DMA_OP(sfx, "6", "0x3000") "s_mov_b32 m0, %0"
#define DMA2_STR(sfx) "s_nop 4\n\ts_mov_b32 %0, m0\n\t" DMA_OP(sfx, "3", "0") DMA_OP(sfx, "4", "0x1000") "s_mov_b32 m0, %0"
template <bool DEV> DI void dma4(unsigned lds, unsigned voff, const char* b0, const char* b1, const char* b2, const char* b3) { unsigned keep;
  if (DEV) asm volatile(DMA4_STR(" sc1") : "=&s"(keep) : "s"(lds), "v"(voff), "s"(b0), "s"(b1), "s"(b2), "s"(b3) : "memory", "scc");
  else asm volatile(DMA4_STR("") : "=&s"(keep) : "s"(lds), "v"(voff), "s"(b0), "s"(b1), "s"(b2), "s"(b3) : "memory", "scc"); }
#define DMA4H_STR(sfx) "s_nop 4\n\ts_mov_b32 %0, m0\n\t" DMA_OP(sfx, "3", "0") DMA_OP(sfx, "4", "0x800") DMA_OP(sfx, "5", "0x1000") DMA_OP(sfx, "6", "0x1800") "s_mov_b32 m0, %0"
template <bool DEV> DI void dma4h(unsigned lds, unsigned voff, const char* b, size_t d) { unsigned keep;
  if (DEV) asm volatile(DMA4H_STR(" sc1") : "=&s"(keep) : "s"(lds), "v"(voff), "s"(b), "s"(b + d), "s"(b + 2 * d), "s"(b + 3 * d) : "memory", "scc");
  else asm volatile(DMA4H_STR("") : "=&s"(keep) : "s"(lds), "v"(voff), "s"(b), "s"(b + d), "s"(b + 2 * d), "s"(b + 3 * d) : "memory", "scc"); }
template <bool DEV> DI void dma2(unsigned lds, unsigned voff, const char* b0, const char* b1) { unsigned keep;
  if (DEV) asm volatile(DMA2_STR(" sc1") : "=&s"(keep) : "s"(lds), "v"(voff), "s"(b0), "s"(b1) : "memory", "scc");
  else asm volatile(DMA2_STR("") : "=&s"(keep) : "s"(lds), "v"(voff), "s"(b0), "s"(b1) : "memory", "scc"); }
DI void dma1(unsigned lds, unsigned voff, const char* b0) { unsigned keep;
  asm volatile("s_nop 4\n\ts_mov_b32 %0, m0\n\ts_mov_b32 m0, %1\n\ts_nop 0\n\tglobal_load_lds_dwordx4 %2, %3\n\ts_mov_b32 m0, %0" : "=&s"(keep) : "s"(lds), "v"(voff), "s"(b0) : "memory"); }
DI void touch_lds(unsigned lds, const char* p) { unsigned keep;
  asm volatile("s_mov_b32 %0, m0\n\ts_mov_b32 m0, %1\n\ts_nop 0\n\tglobal_load_lds_dword %2, off\n\ts_mov_b32 m0, %0" : "=&s"(keep) : "s"(lds), "v"(p) : "memory"); }
constexpr int L_XT = 137216, L_DUMMY = 162048, L_X1 = 147456;
DI unsigned pk2(float a, float b) { f32x2 v = {a, b}; return __builtin_bit_cast(unsigned, __builtin_convertvector(v, bf16x2)); }
DI float bf_lo(unsigned u) { return __uint_as_float(u << 16); }
DI float bf_hi(unsigned u) { return __uint_as_float(u & 0xffff0000u); }
DI float rbf(float a) { return bf_lo(pk2(a, a)); }
DI bf16x8 pack8(const f32x4& a, const f32x4& b) { u32x4 r = {pk2(a[0], a[1]), pk2(a[2], a[3]), pk2(b[0], b[1]), pk2(b[2], b[3])}; return __builtin_bit_cast(bf16x8, r); }
DI f32x4 mfma16(bf16x8 a, bf16x8 b, f32x4 c) { return __builtin_amdgcn_mfma_f32_16x16x32_bf16(a, b, c, 0, 0, 0); }
DI void mfma_acc(f32x4& c, bf16x8 a, bf16x8 b, bool first) {
  if (first) asm volatile("v_mfma_f32_16x16x32_bf16 %0, %1, %2, 0" : "=&v"(c) : "v"(a), "v"(b));
  else asm volatile("v_mfma_f32_16x16x32_bf16 %0, %1, %2, %0" : "+v"(c) : "v"(a), "v"(b)); }
DI void mfma_drain() { SB(); asm volatile("s_nop 7\n\ts_nop 7"); SB(); }
DI s16x4 ldtr(unsigned a) { return __builtin_amdgcn_ds_read_tr16_b64_v4i16((LAS s16x4*)(size_t)a); }
DI bf16x8 cat8(s16x4 lo, s16x4 hi) { return __builtin_bit_cast(bf16x8, __builtin_shufflevector(lo, hi, 0, 1, 2, 3, 4, 5, 6, 7)); }
template <int CTRL> DI float dpp(float x) { return __builtin_bit_cast(float, __builtin_amdgcn_mov_dpp(__builtin_bit_cast(int, x), CTRL, 0xf, 0xf, true)); }
DI float x16max(float x) { auto s = __builtin_amdgcn_permlane16_swap(__float_as_uint(x), __float_as_uint(x), false, false); x = fmaxf(__uint_as_float(s[0]), __uint_as_float(s[1]));
  auto t = __builtin_amdgcn_permlane32_swap(__float_as_uint(x), __float_as_uint(x), false, false); return fmaxf(__uint_as_float(t[0]), __uint_as_float(t[1])); }
DI float x16min(float x) { auto s = __builtin_amdgcn_permlane16_swap(__float_as_uint(x), __float_as_uint(x), false, false); x = fminf(__uint_as_float(s[0]), __uint_as_float(s[1]));
  auto t = __builtin_amdgcn_permlane32_swap(__float_as_uint(x), __float_as_uint(x), false, false); return fminf(__uint_as_float(t[0]), __uint_as_float(t[1])); }
DI float x16sum(float x) { auto s = __builtin_amdgcn_permlane16_swap(__float_as_uint(x), __float_as_uint(x), false, false); x = __uint_as_float(s[0]) + __uint_as_float(s[1]);
  auto t = __builtin_amdgcn_permlane32_swap(__float_as_uint(x), __float_as_uint(x), false, false); return __uint_as_float(t[0]) + __uint_as_float(t[1]); }
DI float sum8(float x) { x += dpp<0xB1>(x); x += dpp<0x4E>(x); x += dpp<0x141>(x); return x; }
DI float sum64(float x) { x = sum8(x); x += dpp<0x128>(x); return x16sum(x); }
DI __amdgpu_buffer_rsrc_t mkrs(const void* p, unsigned bytes) { return __builtin_amdgcn_make_buffer_rsrc((void*)p, 0, bytes, 0x00020000); }
DI void st16_dev(__amdgpu_buffer_rsrc_t r, unsigned off, u32x4 v) { __builtin_amdgcn_raw_buffer_store_b128(v, r, off, 0, 16); }
DI void st8_dev(__amdgpu_buffer_rsrc_t r, unsigned off, u32x2 v) { __builtin_amdgcn_raw_buffer_store_b64(v, r, off, 0, 16); }
DI u32x4 ld16_dev(__amdgpu_buffer_rsrc_t r, unsigned off) { return __builtin_amdgcn_raw_buffer_load_b128(r, off, 0, 16); }
DI unsigned ld4_dev(__amdgpu_buffer_rsrc_t r, unsigned off) { return __builtin_amdgcn_raw_buffer_load_b32(r, off, 0, 16); }
DI void cnt_add(char* ws, int line) { __hip_atomic_fetch_add((GAS unsigned*)(ws + WS_CNT + line * 256), 1u, RLX_AGENT); }
DI void cnt_wait(char* ws, int line, unsigned want) {
  GAS unsigned* w = (GAS unsigned*)(ws + WS_CNT + line * 256); GAS unsigned* tmo = (GAS unsigned*)(ws + WS_CNT + C_TMO * 256);
  for (unsigned spins = 0;;) {
    if ((unsigned)__builtin_amdgcn_readfirstlane(__hip_atomic_load(w, RLX_AGENT)) >= want) break;
    __builtin_amdgcn_s_sleep(1);
    if (++spins > SPIN_CAP) { __hip_atomic_store(tmo, 1u, RLX_AGENT); break; }
    if ((spins & 255u) == 0u && __builtin_amdgcn_readfirstlane(__hip_atomic_load(tmo, RLX_AGENT))) break;
  }
  asm volatile("" ::: "memory");
}
DI void swap16(float& a, float& b) { auto s = __builtin_amdgcn_permlane16_swap(__float_as_uint(a), __float_as_uint(b), false, false); a = __uint_as_float(s[0]); b = __uint_as_float(s[1]); }
DI void swap32(float& a, float& b) { auto s = __builtin_amdgcn_permlane32_swap(__float_as_uint(a), __float_as_uint(b), false, false); a = __uint_as_float(s[0]); b = __uint_as_float(s[1]); }
DI f32x2 lo2(const f32x4& v) { return __builtin_shufflevector(v, v, 0, 1); }
DI f32x2 hi2(const f32x4& v) { return __builtin_shufflevector(v, v, 2, 3); }
DI f32x2 ex2(f32x2 v) { return (f32x2){__builtin_amdgcn_exp2f(v[0]), __builtin_amdgcn_exp2f(v[1])}; }
DI f32x2 up2(unsigned u) { return (f32x2){bf_lo(u), bf_hi(u)}; }
DI unsigned pk2v(f32x2 v) { return __builtin_bit_cast(unsigned, __builtin_convertvector(v, bf16x2)); }
DI f32x2 gelu2(f32x2 x) {
  const float k1 = -2.0f * 0.7978845608028654f * 1.4426950408889634f, k3 = k1 * 0.044715f;
  const f32x2 d = ex2(x * ((x * x) * k3 + k1)) + 1.0f;
  return x * (f32x2){__builtin_amdgcn_rcpf(d[0]), __builtin_amdgcn_rcpf(d[1])};
}
DI int fresh(int v) { asm volatile("" : "+v"(v)); return v; }
template <class T> DI void pinv(T& v) { asm volatile("" : "+v"(v)); }
template <class T> DI void pin(T& v) { asm volatile("" : "+v"(v) :: "memory"); }
DI float gelu_t(float x) {
  const float k1 = -2.0f * 0.7978845608028654f * 1.4426950408889634f, k3 = k1 * 0.044715f;
  const float e = __builtin_amdgcn_exp2f(x * __builtin_fmaf(x * x, k3, k1));
  return x * __builtin_amdgcn_rcpf(1.0f + e);
}

__global__ __launch_bounds__(256) void k_mod(Prm p) {
  __shared__ float red[5][64];
  const int tid = threadIdx.x, lane = tid & 63, wave = tid >> 6, b = blockIdx.x;
  STAMP(0, 0);
  if (tid < 64) ((unsigned*)(p.ws + WS_CNT))[b * 64 + tid] = 0u;
  const int rr = tid >> 3, ch = tid & 7; const bool crit = b < 128;
  const int col = crit ? (b & 31) * 64 : 2048 + (b & 63) * 64, k0 = crit ? (b >> 5) * 256 : ((b >> 6) & 1) * 512;
  u32x4 w[16]; u16 cv[16];
  UNR for (int i = 0; i < 8; ++i) { w[i] = __builtin_nontemporal_load((const u32x4*)(p.w_mod + (size_t)(k0 + rr + 32 * i) * 6144 + col + ch * 8)); cv[i] = p.c[k0 + rr + 32 * i]; }
  f32x2 a[4];
  UNR for (int j = 0; j < 4; ++j) a[j] = (f32x2){0.f, 0.f};
  auto fold = [&](int i) { const float c = bf_lo(cv[i]); const float s = rbf(c / (1.0f + __expf(-c))); UNR for (int j = 0; j < 4; ++j) a[j] += up2(w[i][j]) * s; };
  if (crit) {
    const unsigned dummy = (unsigned)(size_t)(LAS float*)&red[4][0]; const char* q = (const char*)p.wqkv + (size_t)(b * 384 + tid) * 128;
    touch_lds(dummy, q); if (tid < 128) touch_lds(dummy, q + 256 * 128);
    UNR for (int i = 0; i < 8; ++i) fold(i);
  } else {
    UNR for (int i = 8; i < 16; ++i) { w[i] = __builtin_nontemporal_load((const u32x4*)(p.w_mod + (size_t)(k0 + rr + 32 * i) * 6144 + col + ch * 8)); cv[i] = p.c[k0 + rr + 32 * i]; }
    UNR for (int i = 0; i < 16; ++i) fold(i);
  }
  UNR for (int j = 0; j < 4; ++j) UNR for (int e = 0; e < 2; ++e) { float t = a[j][e]; t += dpp<0x128>(t); a[j][e] = x16sum(t); }
  if (lane < 8) { UNR for (int j = 0; j < 4; ++j) { red[wave][lane * 8 + 2 * j] = a[j][0]; red[wave][lane * 8 + 2 * j + 1] = a[j][1]; } }
  __syncthreads();
  if (tid < 64) { const float v = (red[0][tid] + red[1][tid]) + (red[2][tid] + red[3][tid]);
    if (crit) ((float*)(p.ws + WS_MODA))[(b >> 5) * 2048 + col + tid] = v; else ((float*)(p.ws + WS_MODB))[((b >> 6) & 1) * 4096 + (col - 2048) + tid] = v; }
  if (STAMPS) wait_vm<0>();
  STAMP(0, 1);
}
DI f32x4 mod_b(const Prm& p, int idx, bool plus1) {
  const f32x4* pb = (const f32x4*)(p.ws + WS_MODB + (size_t)idx * 4); f32x4 s = pb[0] + pb[1024];
  UNR for (int j = 0; j < 4; ++j) { s[j] = rbf(s[j]); if (plus1) s[j] = rbf(1.0f + s[j]); }
  return s;
}

struct LdAddr { unsigned a, b; };
DI unsigned voff_a(int lw, int lane, int lda) { const int r = 8 * lw + (lane >> 3); return (unsigned)(r * lda * 2 + (((lane & 7) ^ ((r >> 1) & 7)) * 16)); }
DI unsigned voff_b(int lw, int lane, int ldb) { const int rip = lane >> 3, f = ((rip >> 1) & 1) | ((lw & 1) << 1); return (unsigned)((8 * lw + rip) * ldb * 2 + (((lane & 7) ^ (f << 1)) * 16)); }
template <bool DEV> DI void issue_a(unsigned s, const char* ga, int lda, unsigned va) { const size_t d = (size_t)64 * lda; dma4<DEV>(s, va, ga, ga + d, ga + 2 * d, ga + 3 * d); }
template <int NP, bool DEV> DI void issue_b(unsigned s, const char* gb, int ldb, int pstride, unsigned vb) { const size_t d = (size_t)64 * ldb, e = (size_t)2 * pstride;
  if (NP == 1) dma2<DEV>(s + 16384, vb, gb, gb + d);
  else dma4<DEV>(s + 16384, vb, gb, gb + d, gb + e, gb + e + d);
  if (NP == 3) dma2<DEV>(s + 32768, vb, gb + 2 * e, gb + 2 * e + d); }
template <int NST, int PB, int KT, int LAST, bool ALLB, class FA, class FB>
DI void loader_run(FA ia, FB ib) {
  constexpr int P = 4 + PB;
  if (!ALLB) {
    ia(0, 0); ib(1, 1); ia(1, 1); wait_vm<P>(); bar();
    UNR for (int t = 2; t < NST; ++t) { ib(t, t); ia(t, t); }
    int slot = 0;
    for (int b = 1; b <= KT - NST; ++b) { wait_vm<(NST - 2) * P>(); bar(); ia(b - 1 + NST, slot); ib(b - 1 + NST, slot); slot = (slot + 1 == NST) ? 0 : slot + 1; }
    static_assert(ALLB || LAST == KT - NST, "returns after the last issue");
    return;
  }
  ia(0, 0); ia(1, 1);
  wait_vm<4>(); bar();
  UNR for (int t = 2; t < NST; ++t) ia(t, t);
  UNR for (int b = 1; b < NST; ++b) {
    if (b == 1) { wait_vm<(NST - 2) * 4>(); }
    else if (b == 2) { wait_vm<(NST > 2 ? (NST - 3) * 4 + P : 0)>(); }
    else if (b == 3) { wait_vm<(NST > 3 ? (NST - 4) * 4 + 2 * P : 0)>(); }
    else if (b == 4) { wait_vm<(NST > 4 ? (NST - 5) * 4 + 3 * P : 0)>(); }
    else { wait_vm<(NST > 5 ? (NST - 6) * 4 + 4 * P : 0)>(); }
    bar(); ia(b - 1 + NST, b - 1); ib(b - 1 + NST, b - 1); }
  int slot = NST - 1;
  for (int b = NST; b <= KT - NST; ++b) { wait_vm<(NST - 2) * P>(); bar(); ia(b - 1 + NST, slot); ib(b - 1 + NST, slot); slot = (slot + 1 == NST) ? 0 : slot + 1; }
  UNR for (int b = KT - NST + 1; b <= LAST; ++b) { const int al = KT - 1 - b;
    if (al >= 4) wait_vm<4 * P>(); else if (al == 3) wait_vm<3 * P>(); else if (al == 2) wait_vm<2 * P>(); else if (al == 1) wait_vm<P>(); else wait_vm<0>();
    bar(); }
}

template <int NP, int KS, int NST, int NSWAP>
struct Cons {
  static constexpr int MT = 4, NT = 2 * NP, STG = 16384 + NP * 8192, NR1 = MT + 2 * NT, NM1 = MT * NT, NR = NR1 * KS, NM = NM1 * KS, NADV = 4;
  static constexpr int RPS = (NR + NADV <= NM) ? 1 : 2;
  f32x4 acc[MT][NT]; bf16x8 fa[2][KS][MT]; s16x4 fb[2][KS][NT][2]; unsigned ad[4]; int slot;
  DI void init(unsigned sb, int wm, int wn, int lane) {
    const int r = lane & 15, g = lane >> 4, q = r >> 2, p4 = lane & 3, f = ((q >> 1) & 1) | ((g & 1) << 1), arow = wm * 64 + r;
    ad[0] = sb + arow * 128 + ((g ^ ((r >> 1) & 7)) * 16); ad[1] = sb + arow * 128 + (((4 + g) ^ ((r >> 1) & 7)) * 16);
    ad[2] = sb + 16384 + (8 * g + q) * 128 + (((2 * wn) ^ f) * 32) + p4 * 8; ad[3] = sb + 16384 + (8 * g + q) * 128 + (((2 * wn + 1) ^ f) * 32) + p4 * 8;
    slot = 0;
  }
  DI void rd1(int buf, int kb, int ks, int n) {
    if (n == 0) { fa[buf][kb][0] = *(LAS bf16x8*)(size_t)(ad[ks]); return; }
    if (n > 2 * NT) { const int i = n - 2 * NT; fa[buf][kb][i] = *(LAS bf16x8*)(size_t)(ad[ks] + i * 2048); return; }
    const int j = (n - 1) >> 1, hf = (n - 1) & 1;
    fb[buf][kb][j][hf] = ldtr(ad[2 + (j & 1)] + (j >> 1) * 8192 + ks * 4096 + hf * 512);
  }
  DI void mm1(int buf, int kb, int n, bool first) {
    const int i = n / NT, j = n % NT; const bf16x8 b = cat8(fb[buf][kb][j][0], fb[buf][kb][j][1]);
    if (j >= NSWAP) mfma_acc(acc[i][j], fa[buf][kb][i], b, first); else mfma_acc(acc[i][j], b, fa[buf][kb][i], first);
  }
  DI int next_delta() { const bool w = (slot + 1 == NST); slot = w ? 0 : slot + 1; return w ? -(NST - 1) * STG : STG; }
  DI void phase(int use, int ks0, bool rd, bool adv, int delta, bool first = false) {
    UNR for (int n = 0; n < NM; ++n) {
      mm1(use, n / NM1, n % NM1, first && n < NM1); SB();
      UNR for (int u = 0; u < RPS; ++u) { const int x = n * RPS + u;
        if (rd && x < NR) rd1(use ^ 1, x / NR1, ks0 + x / NR1, x % NR1);
        if (adv && x >= NR && x < NR + NADV) { ad[x - NR] += delta; asm volatile("" : "+v"(ad[x - NR])); } }
      SB();
    }
  }
  DI void start() {
    bar();
    UNR for (int n = 0; n < NR; ++n) rd1(0, n / NR1, n / NR1, n % NR1);
    if (KS == 2) { const int d = next_delta(); UNR for (int n = 0; n < NADV; ++n) ad[n] += d; }
    wait_lgkm0(); SB();
  }
  DI void run_half(int kt) {
    { const int d = next_delta(); phase(0, 1, true, true, d, true); wait_lgkm0(); bar(); SB(); phase(1, 0, true, false, 0); wait_lgkm0(); SB(); }
    for (int t = 1; t < kt - 1; ++t) { const int d = next_delta(); phase(0, 1, true, true, d); wait_lgkm0(); bar(); SB(); phase(1, 0, true, false, 0); wait_lgkm0(); SB(); }
    phase(0, 1, true, false, 0); wait_lgkm0(); bar(); SB(); phase(1, 0, false, false, 0); mfma_drain();
  }
  DI void run_first2() {
    bar(); SB(); { const int d = next_delta(); phase(0, 0, true, true, d, true); } wait_lgkm0();
    bar(); SB(); { const int d = next_delta(); phase(1, 0, true, true, d); } wait_lgkm0(); SB();
  }
  DI void run_pairs(int n) {
    for (int t = 0; t < n; t += 2) {
      bar(); SB(); { const int d = next_delta(); phase(0, 0, true, true, d); } wait_lgkm0();
      bar(); SB(); { const int d = next_delta(); phase(1, 0, true, true, d); } wait_lgkm0(); SB();
    }
  }
  DI void run_last2() { bar(); SB(); phase(0, 0, true, false, 0); wait_lgkm0(); SB(); phase(1, 0, false, false, 0); mfma_drain(); }
};

struct ConsG {
  static constexpr int STG = 32768;
  f32x4 acc[4][4], xa[2]; bf16x8 fa[2][2][4], fx[2]; s16x4 fb[2][2][4][2]; unsigned ad[6], xad, xb; int slot, xo;
  DI void init(unsigned sb, unsigned xtab, int t0, int wm, int wn, int lane) {
    const int r = lane & 15, g = lane >> 4, q = r >> 2, p4 = lane & 3, f = ((q >> 1) & 1) | ((g & 1) << 1), arow = wm * 64 + r;
    ad[0] = sb + arow * 128 + ((g ^ ((r >> 1) & 7)) * 16); ad[1] = sb + arow * 128 + (((4 + g) ^ ((r >> 1) & 7)) * 16);
    UNR for (int j = 0; j < 4; ++j) ad[2 + j] = sb + 16384 + ((j >> 1) ^ wm) * 8192 + (8 * g + q) * 128 + (((2 * wn + (j & 1)) ^ f) * 32) + p4 * 8;
    xb = xtab + (r & 1) * 2112 + g * 16; xo = t0 * 128; xad = xb + xo; slot = 0;
  }
  DI void rd1(int buf, int ks, int n) {
    if (n == 0) { fa[buf][ks][0] = *(LAS bf16x8*)(size_t)(ad[ks]); return; }
    if (n > 8) { fa[buf][ks][n - 8] = *(LAS bf16x8*)(size_t)(ad[ks] + (n - 8) * 2048); return; }
    const int j = (n - 1) >> 1, hf = (n - 1) & 1;
    fb[buf][ks][j][hf] = ldtr(ad[2 + j] + ks * 4096 + hf * 512);
  }
  DI int next_delta() { const bool w = (slot == 2); slot = w ? 0 : slot + 1; return w ? -2 * STG : STG; }
  template <bool X> DI void phase(int use, bool rd, bool adv, bool first) {
    const int delta = adv ? next_delta() : 0; if (X) xo = (xo + 128) & 2047;
    UNR for (int n = 0; n < (X ? 36 : 32); ++n) {
      if (n < 32) { const int ks = n >> 4, i = (n >> 2) & 3, j = n & 3; mfma_acc(acc[i][j], cat8(fb[use][ks][j][0], fb[use][ks][j][1]), fa[use][ks][i], first && n < 16); }
      else { const int ks = (n >> 1) & 1, j = n & 1; mfma_acc(xa[j], cat8(fb[use][ks][j][0], fb[use][ks][j][1]), fx[ks], first && n < 34); }
      SB();
      const int x = X ? n - 2 : n;
      if (X && n < 2) fx[n] = *(LAS bf16x8*)(size_t)(xad + n * 64);
      if (rd && x >= 0 && x < 24) rd1(use ^ 1, x / 12, x % 12);
      if (adv && x >= 24 && x < 30) { ad[x - 24] += delta; pinv(ad[x - 24]); }
      if (X && x == 30) { xad = xb + xo; pinv(xad); }
      SB();
    }
  }
  DI void start() { bar(); UNR for (int n = 0; n < 24; ++n) rd1(0, n / 12, n % 12);
    const int d = next_delta(); UNR for (int n = 0; n < 6; ++n) ad[n] += d;
    wait_lgkm0(); SB(); }
  template <bool X> DI void run(int n, bool first) {
    if (first) { bar(); SB(); phase<X>(0, true, true, true); wait_lgkm0(); bar(); SB(); phase<X>(1, true, true, false); wait_lgkm0(); SB(); n -= 2; }
    for (int t = 0; t < n; t += 2) { bar(); SB(); phase<X>(0, true, true, false); wait_lgkm0(); bar(); SB(); phase<X>(1, true, true, false); wait_lgkm0(); SB(); }
  }
  DI void run_last2() { bar(); SB(); phase<false>(0, true, false, false); wait_lgkm0(); SB(); phase<false>(1, false, false, false); mfma_drain(); }
};

template <int NP2, int NC> DI f32x2 stats_comb(u32x4 (&v)[NP2]) {
  float m = 0.f; UNR for (int i = 0; i < NP2; ++i) m += __uint_as_float(v[i][0]) + __uint_as_float(v[i][2]);
  m *= 1.0f / (2 * NP2); float q = 0.f;
  UNR for (int i = 0; i < NP2; ++i) { const float d0 = __uint_as_float(v[i][0]) - m, d1 = __uint_as_float(v[i][2]) - m; q += (__uint_as_float(v[i][1]) + __uint_as_float(v[i][3])) + (float)NC * (d0 * d0 + d1 * d1); }
  return (f32x2){m, __builtin_amdgcn_rsqf(q * (1.0f / (2 * NC * NP2)) + 1e-5f)};
}

struct Pre { u32x4 xv[2]; };
constexpr int L_MT2 = 163328, L_MT4 = 139264;
DI void pre_load(const Prm& p, int tid, Pre& q) {
  const int rb = ((blockIdx.x & 7) >> 1) * 4 + (blockIdx.x >> 6), col = ((blockIdx.x & 1) * 8 + ((blockIdx.x >> 3) & 7)) * 64 + (tid & 7) * 8;
  UNR for (int i = 0; i < 2; ++i) q.xv[i] = __builtin_nontemporal_load((const u32x4*)(p.x + (size_t)(rb * 128 + (tid >> 3) + 64 * i) * D_ + col));
}
DI f32x4 tab_load(const Prm& p, int lane, int o0, int o1) { const int col = ((blockIdx.x & 1) * 8 + ((blockIdx.x >> 3) & 7)) * 64;
  return *(const f32x4*)(p.ws + WS_MODB + (size_t)((lane >> 5) * 4096 + ((lane & 16) ? o1 : o0) + col + (lane & 15) * 4) * 4); }
DI void tab_store(LAS char* t, int lane, f32x4 v) {
  UNR for (int j = 0; j < 4; ++j) { float a = v[j], b = a; swap32(a, b); a = rbf(a + b); v[j] = (lane & 16) ? rbf(1.0f + a) : a; }
  if (lane < 32) *(LAS f32x4*)(t + lane * 16) = v;
}
template <bool LAST> DI void proj_ib(const Prm& p, unsigned sb, int lw, int lane, int t, int slot) {
  constexpr int KT = LAST ? 28 : 16; const int idx = blockIdx.x >> 3, cb = (blockIdx.x & 1) * 8 + (idx & 7); t += (idx >> 3) * (KT / 4); if (t >= KT) t -= KT;
  issue_b<1, false>(sb + slot * 24576 + lw * 1024, (const char*)(LAST ? p.w_out : p.wo) + cb * 128 + (size_t)t * 64 * D_ * 2, D_, 64, voff_b(lw, lane, D_));
}
DI void gmlp_ib0(const Prm& p, unsigned sb, int lw, int lane, int t) {
  const int idx = blockIdx.x >> 3, cb = (blockIdx.x & 1) * 7 + idx % 7;
  issue_b<2, false>(sb + t * 32768 + lw * 1024, (const char*)p.w_in + cb * 256 + (size_t)((t + 4 * (idx / 7)) & 15) * 64 * 3584 * 2 + FH * 2, 3584, 64, voff_b(lw, lane, 3584));
}
template <class F> DI void publish_and_open(const Prm& p, int wave, int tid, int line, bool open, F issue) {
  bar();
  if (wave >= 4 && open) { issue(0); wait_vm<8>(); } else wait_vm<0>();
  bar();
  if (tid == 0 && line >= 0) cnt_add(p.ws, line);
  if (wave >= 4 && open) issue(1);
}
DI void softmax_pm2(const f32x4 (&p)[2][2], u32x2 (&o)[2][4]) {
  const float L2E = 1.4426950408889634f; float m[2], m2[2]; f32x2 a[2][4], b[2][4];
  UNR for (int u = 0; u < 2; ++u) { const f32x4 &p0 = p[u][0], &p1 = p[u][1];
    m[u] = fmaxf(fmaxf(fmaxf(fmaxf(p0[0], p0[1]), p0[2]), fmaxf(fmaxf(p0[3], p1[0]), p1[1])), fmaxf(p1[2], p1[3]));
    m2[u] = -fminf(fminf(fminf(fminf(p0[0], p0[1]), p0[2]), fminf(fminf(p0[3], p1[0]), p1[1])), fminf(p1[2], p1[3])); }
  UNR for (int u = 0; u < 2; ++u) { swap16(m[u], m2[u]); m[u] = fmaxf(m[u], m2[u]); m2[u] = m[u]; }
  UNR for (int u = 0; u < 2; ++u) { swap32(m[u], m2[u]); m[u] = fmaxf(m[u], m2[u]) * -L2E; m2[u] = m[u]; }
  UNR for (int u = 0; u < 2; ++u) swap16(m[u], m2[u]);
  SB();
  UNR for (int u = 0; u < 2; ++u) UNR for (int i = 0; i < 4; ++i) { const f32x2 v = (i & 1) ? hi2(p[u][i >> 1]) : lo2(p[u][i >> 1]); a[u][i] = v * L2E + m[u]; b[u][i] = v * -L2E + m2[u]; }
  SB();
  UNR for (int u = 0; u < 2; ++u) UNR for (int i = 0; i < 4; ++i) { a[u][i] = ex2(a[u][i]); b[u][i] = ex2(b[u][i]); }
  SB();
  UNR for (int u = 0; u < 2; ++u) { const f32x2 sa = (a[u][0] + a[u][1]) + (a[u][2] + a[u][3]), sb = (b[u][0] + b[u][1]) + (b[u][2] + b[u][3]); m[u] = sa[0] + sa[1]; m2[u] = sb[0] + sb[1]; }
  UNR for (int u = 0; u < 2; ++u) { swap16(m[u], m2[u]); m[u] += m2[u]; m2[u] = m[u]; }
  UNR for (int u = 0; u < 2; ++u) { swap32(m[u], m2[u]); m[u] = __builtin_amdgcn_rcpf(m[u] + m2[u]); m2[u] = m[u]; }
  UNR for (int u = 0; u < 2; ++u) swap16(m[u], m2[u]);
  SB();
  UNR for (int u = 0; u < 2; ++u) UNR for (int i = 0; i < 4; ++i) { a[u][i] *= m[u]; b[u][i] *= m2[u]; }
  UNR for (int u = 0; u < 2; ++u) UNR for (int i = 0; i < 2; ++i) { o[u][i] = (u32x2){pk2v(a[u][2 * i]), pk2v(a[u][2 * i + 1])}; o[u][2 + i] = (u32x2){pk2v(b[u][2 * i]), pk2v(b[u][2 * i + 1])}; }
}
DI bf16x8 cat8u(u32x2 a, u32x2 b) { return __builtin_bit_cast(bf16x8, (u32x4){a[0], a[1], b[0], b[1]}); }
template <bool PUB>
DI void phase_qkv(const Prm& p, LAS char* smem, int wave, int lane, int tid, Pre& pr) {
  constexpr int NST = 3, KT = 16, L_PHI = 0, L_XKV = 34816, L_OST = 65536, L_WPHI = 122880, L_HX = 126976, L_XZ = 151552, L_KVS = 153600, L_ZS = 161792, L_TAB = 126976;
  const int xcd = blockIdx.x & 7, idx = blockIdx.x >> 3, rb = (xcd >> 2) * 8 + (idx >> 2), hp = (xcd & 3) * 4 + (idx & 3), t0 = 2 * (idx >> 2);
  const unsigned sb = (unsigned)(size_t)smem; const int r = lane & 15, g = lane >> 4, wm = (wave >> 1) & 1, wn = wave & 1;
  const int lw = wave & 3; const char* B = (const char*)p.wqkv + hp * 128; const unsigned vb = voff_b(lw, lane, 3072);
  auto ib = [&](int t, int slot) { issue_b<3, false>(sb + slot * 40960 + lw * 1024, B + (size_t)((t + t0) & 15) * 64 * 3072 * 2, 3072, 1024, vb); };
  STAMP(1, 0);
  {
    const int row = rb * 128 + hp * 8 + wave; u32x4 xv[2]; f32x4 pa[4];
    UNR for (int i = 0; i < 2; ++i) xv[i] = __builtin_nontemporal_load((const u32x4*)(p.x + (size_t)row * D_ + i * 512 + lane * 8));
    UNR for (int k = 0; k < 4; ++k) pa[k] = ((const f32x4*)(p.ws + WS_MODA))[k * 512 + tid];
    { f32x4 s4 = (pa[0] + pa[1]) + (pa[2] + pa[3]);
      UNR for (int j = 0; j < 4; ++j) { s4[j] = rbf(s4[j]); if (wave >= 4) s4[j] = rbf(1.0f + s4[j]); }
      *(LAS f32x4*)(smem + L_TAB + tid * 16) = s4; }
    f32x2 v[8]; UNR for (int j = 0; j < 8; ++j) v[j] = up2(xv[j >> 2][j & 3]);
    const f32x2 t = ((v[0] + v[1]) + (v[2] + v[3])) + ((v[4] + v[5]) + (v[6] + v[7])); const float mu = sum64(t[0] + t[1]) * (1.0f / 1024.0f); f32x2 q2 = {0.f, 0.f};
    UNR for (int j = 0; j < 8; ++j) { v[j] -= mu; q2 += v[j] * v[j]; }
    const float rs = __builtin_amdgcn_rsqf(sum64(q2[0] + q2[1]) * (1.0f / 1024.0f) + 1e-5f);
    wait_lgkm0(); bar();
    STAMPL(1, 3);
    const __amdgpu_buffer_rsrc_t rh = mkrs(p.ws + WS_H, 4u << 20);
    UNR for (int i = 0; i < 2; ++i) { const int c0 = i * 512 + lane * 8; LAS char* tb = smem + L_TAB + c0 * 4;
      const f32x4 s0 = *(LAS f32x4*)tb, s1 = *(LAS f32x4*)(tb + 16), c0v = *(LAS f32x4*)(tb + 4096), c1v = *(LAS f32x4*)(tb + 4112);
      const f32x2 sh[4] = {lo2(s0), hi2(s0), lo2(s1), hi2(s1)}, sc[4] = {lo2(c0v), hi2(c0v), lo2(c1v), hi2(c1v)}; u32x4 o;
      UNR for (int j = 0; j < 4; ++j) o[j] = pk2v((v[4 * i + j] * rs) * sc[j] + sh[j]);
      st16_dev(rh, (unsigned)(row * D_ + c0) * 2, o); }
    STAMPL(1, 1);
    wait_vm<0>(); bar();
    STAMPL(1, 4);
    if (tid == 0) cnt_add(p.ws, C_H + rb);
    if (wave >= 4) { dma1(sb + L_WPHI + lw * 1024, lane * 16, (const char*)p.wphi + hp * 4096 + lw * 1024); UNR for (int t2 = 0; t2 < (G1_ALLB ? NST : 1); ++t2) ib(t2, t2); }
    if (wave == 0) cnt_wait(p.ws, C_H + rb, 16u);
    bar();
  }
  STAMP(1, 2);
  const int q = r >> 2, p4 = lane & 3; const f32x4 z4 = {0.f, 0.f, 0.f, 0.f};
  const __amdgpu_buffer_rsrc_t rk = mkrs(p.ws + WS_KVP, 2u << 20), rz = mkrs(p.ws + WS_ZP, 131072);
  LAS char* hx = smem + L_HX + lw * 4096 + lane * 16;
  bf16x8 pf[4][2];
  auto load_wf = [&](bf16x8 (&wf)[2]) { UNR for (int ft = 0; ft < 2; ++ft) { const unsigned a = sb + L_WPHI + wn * 2048 + (4 * g + q) * 64 + ft * 32 + p4 * 8; wf[ft] = cat8(ldtr(a), ldtr(a + 1024)); } };
  if (wave >= 4) {
    const char* A = p.ws + WS_H + (size_t)rb * 128 * D_ * 2; const unsigned va = voff_a(lw, lane, D_);
    __builtin_amdgcn_s_setprio(3);
    loader_run<NST, 6, KT, KT - 3, G1_ALLB>([&](int t, int slot) { issue_a<true>(sb + slot * 40960 + lw * 1024, A + ((t + t0) & 15) * 128, D_, va); }, ib);
    { const int l = blockIdx.x * 176 + lw * 44 + (lane < 44 ? lane : 0), l2 = l - 16384;
      touch_lds(sb + L_DUMMY, l2 < 0 ? (const char*)p.wo + (size_t)l * 128 : (const char*)p.w_in + (size_t)(l2 / 28) * 7168 + 3584 + (l2 % 28) * 128); }
    wait_vm<11>(); bar(); wait_vm<1>(); bar(); bar();
    __builtin_amdgcn_s_setprio(0);
    bf16x8 wf[2]; load_wf(wf);
    bar();
    auto qunit = [&](int mt) { f32x4 pp[2][2]; u32x2 o[2][4];
      UNR for (int u = 0; u < 2; ++u) { const bf16x8 qf = *(LAS bf16x8*)(hx + (mt + u) * 1024); pp[u][0] = mfma16(wf[0], qf, z4); pp[u][1] = mfma16(wf[1], qf, z4); }
      softmax_pm2(pp, o);
      UNR for (int u = 0; u < 2; ++u) { pf[mt + u][0] = cat8u(o[u][0], o[u][1]); pf[mt + u][1] = cat8u(o[u][2], o[u][3]); pin(pf[mt + u][0]); pin(pf[mt + u][1]); } };
    bar();
    qunit(0);
    bar();
    qunit(2);
  } else {
    STAMP(2, 0); STAMPC(0); Cons<3, 1, NST, 4> c; c.init(sb, wm, wn, lane); c.start(); STAMP(2, 1); STAMPC(1); c.run_half(KT); STAMP(2, 2); STAMPC(2);
    bf16x8 wf[2]; load_wf(wf);
    UNR for (int mt = 0; mt < 4; ++mt) *(LAS bf16x8*)(hx + mt * 1024) = pack8(c.acc[mt][0], c.acc[mt][1]);
    wait_lgkm0(); bar();
    STAMP(0, 2);
    LAS char* phi = smem + L_PHI + wave * (64 * 136);
    __builtin_amdgcn_s_setprio(2);
    UNR for (int mt = 0; mt < 4; mt += 2) { f32x4 pp[2][2]; u32x2 o[2][4];
      UNR for (int u = 0; u < 2; ++u) { const bf16x8 kf = pack8(c.acc[mt + u][2], c.acc[mt + u][3]); pp[u][0] = mfma16(wf[0], kf, z4); pp[u][1] = mfma16(wf[1], kf, z4); }
      softmax_pm2(pp, o);
      UNR for (int u = 0; u < 2; ++u) UNR for (int ft = 0; ft < 4; ++ft) *(LAS u32x2*)(phi + (16 * (mt + u) + r) * 136 + (16 * ft + 4 * g) * 2) = o[u][ft]; }
    STAMP(0, 3);
    f32x4 kv[4][2], zz[4];
    { const u32x4 o4 = {0x3f803f80u, 0x3f803f80u, 0x3f803f80u, 0x3f803f80u}; const bf16x8 ones = __builtin_bit_cast(bf16x8, o4);
      UNR for (int kk = 0; kk < 2; ++kk) { const bf16x8 v0 = pack8(c.acc[2 * kk][4], c.acc[2 * kk + 1][4]), v1 = pack8(c.acc[2 * kk][5], c.acc[2 * kk + 1][5]);
        UNR for (int ft = 0; ft < 4; ++ft) { const unsigned a = (unsigned)(size_t)phi + (32 * kk + 4 * g + q) * 136 + ft * 32 + p4 * 8; const bf16x8 pk = cat8(ldtr(a), ldtr(a + 16 * 136));
          kv[ft][0] = mfma16(pk, v0, kk ? kv[ft][0] : z4); kv[ft][1] = mfma16(pk, v1, kk ? kv[ft][1] : z4); zz[ft] = mfma16(pk, ones, kk ? zz[ft] : z4); } } }
    LAS char* xk = smem + L_XKV + lane * 16;
    if (wm) { UNR for (int ft = 0; ft < 4; ++ft) { *(LAS f32x4*)(xk + (wave * 4 + ft) * 1024) = kv[ft][0]; kv[ft][0] = kv[ft][1]; } }
    else { UNR for (int ft = 0; ft < 4; ++ft) *(LAS f32x4*)(xk + (wave * 4 + ft) * 1024) = kv[ft][1]; }
    if (r == 0) { UNR for (int ft = 0; ft < 4; ++ft) *(LAS f32x4*)(smem + L_XZ + wave * 256 + (16 * ft + 4 * g) * 4) = zz[ft]; }
    STAMP(0, 4);
    wait_lgkm0(); bar();
    STAMP(0, 5);
    const int slotid = (2 * hp + wn) * 16 + rb;
    UNR for (int k2 = 0; k2 < 2; ++k2) { const f32x4 a = kv[2 * k2][0] + *(LAS f32x4*)(xk + ((wave ^ 2) * 4 + 2 * k2) * 1024), b = kv[2 * k2 + 1][0] + *(LAS f32x4*)(xk + ((wave ^ 2) * 4 + 2 * k2 + 1) * 1024);
      st16_dev(rk, (unsigned)(slotid * 4 + wm * 2 + k2) * 1024 + lane * 16, __builtin_bit_cast(u32x4, pack8(a, b))); }
    if (wm == 0 && lane < 16) st16_dev(rz, (unsigned)(slotid * 64 + lane * 4) * 4, __builtin_bit_cast(u32x4, *(LAS f32x4*)(smem + L_XZ + wn * 256 + lane * 16) + *(LAS f32x4*)(smem + L_XZ + (2 + wn) * 256 + lane * 16)));
    STAMP(2, 3); STAMPC(3);
    __builtin_amdgcn_s_setprio(0);
    wait_vm<0>(); bar();
    if (tid == 0) cnt_add(p.ws, C_KV + hp);
    STAMP(2, 4); STAMPC(4);
    if (wave == 0) cnt_wait(p.ws, C_KV + hp, 16u);
    if (wave == 3) { tab_store(smem + L_MT2, lane, tab_load(p, lane, 0, 2048)); wait_lgkm0(); }
    STAMP(2, 5); STAMPC(5);
  }
  bar();
  STAMP(2, 6); STAMPC(6);
  {
    const int hl = wave >> 2, ck = wave & 3, head = 2 * hp + hl;
    u32x4 v[16]; UNR for (int i = 0; i < 16; ++i) v[i] = ld16_dev(rk, (unsigned)((head * 16 + ((i + t0) & 15)) * 4 + ck) * 1024 + lane * 16);
    if (ck == 0) { float zs = 0.f; unsigned zv[16]; UNR for (int i = 0; i < 16; ++i) zv[i] = ld4_dev(rz, (unsigned)((head * 16 + i) * 64 + lane) * 4);
      UNR for (int i = 0; i < 16; ++i) zs += __uint_as_float(zv[i]);
      *(LAS u16*)(smem + L_ZS + hl * 128 + lane * 2) = (u16)pk2(zs, zs); }
    f32x2 a[4]; UNR for (int j = 0; j < 4; ++j) a[j] = up2(v[0][j]);
    UNR for (int i = 1; i < 16; ++i) UNR for (int j = 0; j < 4; ++j) a[j] += up2(v[i][j]);
    *(LAS u32x4*)(smem + L_KVS + hl * 4096 + ck * 1024 + lane * 16) = (u32x4){pk2v(a[0]), pk2v(a[1]), pk2v(a[2]), pk2v(a[3])};
    wait_lgkm0(); bar();
    STAMP(2, 7); STAMPC(7);
  }
  pre_load(p, tid, pr);
  asm volatile("" ::: "memory");
  if (wave >= 4) { bf16x8 kv[2][2], zf[2];
    UNR for (int dt = 0; dt < 2; ++dt) UNR for (int k2 = 0; k2 < 2; ++k2) kv[dt][k2] = *(LAS bf16x8*)(smem + L_KVS + wn * 4096 + (dt * 2 + k2) * 1024 + lane * 16);
    UNR for (int k2 = 0; k2 < 2; ++k2) { LAS char* a = smem + L_ZS + wn * 128 + (32 * k2 + 4 * g) * 2; zf[k2] = cat8(*(LAS s16x4*)a, *(LAS s16x4*)(a + 32)); }
    UNR for (int mt = 0; mt < 4; ++mt) {
      const f32x4 den = mfma16(zf[1], pf[mt][1], mfma16(zf[0], pf[mt][0], z4)); const float rd = __builtin_amdgcn_rcpf(den[0] + 1e-6f);
      UNR for (int dt = 0; dt < 2; ++dt) { const f32x4 nu = mfma16(kv[dt][1], pf[mt][1], mfma16(kv[dt][0], pf[mt][0], z4)) * rd;
        *(LAS u32x2*)(smem + L_OST + (64 * wm + 16 * mt + r) * 144 + (32 * wn + 16 * dt + 4 * g) * 2) = (u32x2){pk2(nu[0], nu[1]), pk2(nu[2], nu[3])}; }
    }
    wait_lgkm0(); }
  bar();
  { const __amdgpu_buffer_rsrc_t ro = mkrs(p.ws + WS_O, 4u << 20);
    UNR for (int i = 0; i < 2; ++i) { const int row = (tid >> 3) + 64 * i, ch = tid & 7;
      st16_dev(ro, (unsigned)((rb * 128 + row) * D_ + hp * 64 + ch * 8) * 2, *(LAS u32x4*)(smem + L_OST + row * 144 + ch * 16)); } }
  STAMP(2, 8); STAMPC(8);
  if (PUB) publish_and_open(p, wave, tid, C_O + rb, true, [&](int h) { UNR for (int t = 4 * h; t < 4 + 2 * h; ++t) proj_ib<false>(p, sb, lw, lane, t, t); });
  STAMP(2, 9); STAMPC(9);
}

template <bool LAST, bool WAIT, bool PUB>
DI void phase_proj(const Prm& p, LAS char* smem, int wave, int lane, int tid, Pre& pr) {
  constexpr int NST = 6, KT = LAST ? 28 : 16, LDA = LAST ? FH : D_;
  const int xcd = blockIdx.x & 7, idx = blockIdx.x >> 3, rb = (xcd >> 1) * 4 + (idx >> 3), cb = (xcd & 1) * 8 + (idx & 7), t0 = (idx >> 3) * (KT / 4);
  const unsigned sb = (unsigned)(size_t)smem; const int r = lane & 15, g = lane >> 4, wm = (wave >> 1) & 1, wn = wave & 1;
  const int ch = tid & 7, col = cb * 64 + ch * 8; u32x4 (&xv)[2] = pr.xv; f32x4 gv[2], cv[2];
  auto tab_read = [&]() { LAS char* t = smem + (LAST ? L_MT4 : L_MT2) + ch * 32; UNR for (int i = 0; i < 2; ++i) { gv[i] = *(LAS f32x4*)(t + 16 * i); if (!LAST) cv[i] = *(LAS f32x4*)(t + 256 + 16 * i); } wait_lgkm0(); };
  if (WAIT) { if (wave == 0) cnt_wait(p.ws, (LAST ? C_U : C_O) + rb, LAST ? 14u : 16u); bar(); }
  if (wave >= 4) {
    const int lw = wave - 4; const char* A = p.ws + (LAST ? WS_U : WS_O) + (size_t)rb * 128 * LDA * 2; const unsigned va = voff_a(lw, lane, LDA);
    auto rot = [&](int t) { t += t0; return t >= KT ? t - KT : t; };
    auto ib = [&](int t, int slot) { proj_ib<LAST>(p, sb, lw, lane, t, slot); };
    if (!WAIT) { UNR for (int t = 0; t < NST; ++t) ib(t, t); }
    __builtin_amdgcn_s_setprio(3);
    loader_run<NST, 2, KT, KT - 3, true>([&](int t, int slot) { issue_a<true>(sb + slot * 24576 + lw * 1024, A + rot(t) * 128, LDA, va); }, ib);
    __builtin_amdgcn_s_setprio(0);
    f32x4 xr[2][4];
    if (!LAST && wave >= 6) { UNR for (int h = 0; h < 2; ++h) UNR for (int e = 0; e < 4; ++e) xr[h][e] = *(const f32x4*)(p.ws + WS_MODB + (size_t)((e >> 1) * 4096 + (h ? 1024 : 2048) + (tid - 384) * 8 + (e & 1) * 4) * 4); }
    wait_vm<6>(); bar(); wait_vm<0>(); bar();
    u32x4 xt[2];
    if (!LAST && wave >= 6) { UNR for (int h = 0; h < 2; ++h) { f32x4 a = xr[h][0] + xr[h][2], b = xr[h][1] + xr[h][3];
        if (!h) { UNR for (int e = 0; e < 4; ++e) { a[e] = 1.0f + rbf(a[e]); b[e] = 1.0f + rbf(b[e]); } }
        xt[h] = __builtin_bit_cast(u32x4, pack8(a, b)); pin(xt[h]); } }
    bar();
    if (!LAST && wave >= 6) { UNR for (int h = 0; h < 2; ++h) *(LAS u32x4*)(smem + L_XT + h * 2112 + (tid - 384) * 16) = xt[h]; }
    tab_read();
  } else {
    STAMP(2, LAST ? 26 : 10); STAMPC(LAST ? 26 : 10);
    Cons<1, 2, NST, 2> c; c.init(sb, wm, wn, lane); c.start(); STAMP(2, LAST ? 27 : 11); STAMPC(LAST ? 27 : 11); c.run_first2();
    f32x4 tv; if (LAST && wave == 0) tv = tab_load(p, fresh(tid) & 63, 3072, 3072);
    c.run_pairs(KT - 4);
    if (LAST && wave == 0) { tab_store(smem + L_MT4, fresh(tid) & 63, tv); wait_lgkm0(); SB(); }
    c.run_last2(); STAMP(2, LAST ? 28 : 12); STAMPC(LAST ? 28 : 12);
    bar();
    tab_read();
    UNR for (int mt = 0; mt < 4; ++mt) UNR for (int j = 0; j < 2; ++j)
      *(LAS u32x2*)(smem + (64 * wm + 16 * mt + r) * 144 + (32 * wn + 16 * j + 4 * g) * 2) = (u32x2){pk2(c.acc[mt][j][0], c.acc[mt][j][1]), pk2(c.acc[mt][j][2], c.acc[mt][j][3])};
    wait_lgkm0();
  }
  bar();
  const f32x2 g2[4] = {lo2(gv[0]), hi2(gv[0]), lo2(gv[1]), hi2(gv[1])};
  u32x4 o[2];
  UNR for (int i = 0; i < 2; ++i) {
    const int row = (tid >> 3) + 64 * i; const u32x4 av = *(LAS u32x4*)(smem + row * 144 + ch * 16);
    if (LAST) xv[i] = *(LAS u32x4*)(smem + L_X1 + i * 8192 + tid * 16);
    UNR for (int j = 0; j < 4; ++j) o[i][j] = pk2v(up2(xv[i][j]) + up2(pk2v(g2[j] * up2(av[j]))));
    if (!LAST) *(LAS u32x4*)(smem + L_X1 + i * 8192 + tid * 16) = o[i];
    const size_t off = (size_t)(rb * 128 + row) * D_ + col;
    if (LAST) *(u32x4*)(p.out + off) = o[i];
    else {
      const f32x2 c2[4] = {lo2(cv[0]), hi2(cv[0]), lo2(cv[1]), hi2(cv[1])}; f32x2 v[4]; u32x4 y;
      UNR for (int j = 0; j < 4; ++j) { v[j] = up2(o[i][j]); y[j] = pk2v(v[j] * c2[j]); }
      st16_dev(mkrs(p.ws + WS_Y, 4u << 20), (unsigned)off * 2, y);
      const f32x2 t = (v[0] + v[1]) + (v[2] + v[3]); const float mu = sum8(t[0] + t[1]) * (1.0f / 64.0f); f32x2 q = {0.f, 0.f};
      UNR for (int j = 0; j < 4; ++j) { const f32x2 d = v[j] - mu; q += d * d; }
      const float qs = sum8(q[0] + q[1]);
      if (ch == 0) st8_dev(mkrs(p.ws + WS_ST1, 262144), (unsigned)(((cb >> 1) * 2048 + rb * 128 + row) * 16 + (cb & 1) * 8), (u32x2){__float_as_uint(mu), __float_as_uint(qs)});
    }
  }
  STAMP(2, LAST ? 29 : 13); STAMPC(LAST ? 29 : 13);
  if (PUB) publish_and_open(p, wave, tid, C_Y + rb, idx < 28, [&](int h) { UNR for (int t = 2 * h; t < 2 + h; ++t) gmlp_ib0(p, sb, wave & 3, lane, t); });
  if (STAMPS && LAST) wait_vm<0>();
  STAMP(2, LAST ? 30 : 14); STAMPC(LAST ? 30 : 14);
}

DI void helper_touch(const Prm& p, unsigned dummy, int hj, int wave, int tid) {
  const unsigned long long t0 = __builtin_amdgcn_s_memrealtime();
  for (int i = 0; i < 8; ++i) { int l = (((i < 2 ? i : i < 6 ? i - 2 : i - 6) * 32 + hj) * 512) + tid; if ((i < 2 || i >= 6) && l >= 28672) l -= 16384;
    const char* a = i < 2 ? (const char*)p.w_in + (size_t)(l / 28) * 7168 + (l % 28) * 128 : i < 6 ? (const char*)p.w_sp + (size_t)(l & 2047) * 4096 + (l >> 11) * 128 : (const char*)p.w_out + (size_t)l * 128;
    while (__builtin_amdgcn_s_memrealtime() - t0 < (unsigned long long)(PACE * (i * 8 + wave))) __builtin_amdgcn_s_sleep(4);
    touch_lds(dummy, a); wait_vm<1>(); }
  wait_vm<0>();
  STAMP(0, 14);
}
template <bool WAIT, bool PUB>
DI void phase_gmlp(const Prm& p, LAS char* smem, int wave, int lane, int tid) {
  constexpr int NST = 3, L_IMG = 98304, L_TB1 = 131072, L_TB2 = 132096, L_TCS = 133120, L_ST = 135168, L_G = 32768;
  const int xcd = blockIdx.x & 7, idx = blockIdx.x >> 3;
  if (idx >= 28) { const int j = (idx - 28) * 8 + xcd; helper_touch(p, (unsigned)(size_t)smem + 65536, j, wave, tid);
    if (PUB) publish_and_open(p, wave, tid, -1, true, [&](int h) { UNR for (int t = 4 * h; t < 4 + 2 * h; ++t) proj_ib<true>(p, (unsigned)(size_t)smem, wave & 3, lane, t, t); });
    return; }
  const int rb = (xcd >> 1) * 4 + idx / 7, cb = (xcd & 1) * 7 + idx % 7, t0 = 4 * (idx / 7);
  auto chunk_at = [&](int base, int row, int ch) { return (LAS u32x4*)(smem + base + row * 256 + ((ch ^ (row & 15)) * 16)); };
  auto normalize = [&]() {
    const int tid = fresh(threadIdx.x); const __amdgpu_buffer_rsrc_t ro = mkrs(p.ws + WS_Z2N, 8u << 20);
    UNR for (int h = 0; h < 2; ++h) { u32x4 v[4]; f32x2 st[4];
      UNR for (int i = 0; i < 4; ++i) { const int row = 64 * h + 16 * i + (tid >> 4); v[i] = *chunk_at(L_IMG, row, tid & 15); st[i] = *(LAS f32x2*)(smem + L_TB2 + row * 8); }
      UNR for (int i = 0; i < 4; ++i) { UNR for (int e = 0; e < 4; ++e) v[i][e] = pk2v(up2(v[i][e]) * st[i][0] + st[i][1]);
        st16_dev(ro, (unsigned)((rb * 128 + 64 * h + 16 * i + (tid >> 4)) * FH + cb * 128 + (tid & 15) * 8) * 2, v[i]); } } };
  float bs[4];
  if (wave >= 6) {
    const int row = tid - 384; const unsigned so = (unsigned)(rb * 128 + row) * 16;
    auto bars = [&](int n) { for (int i = 0; i < n; ++i) bar(); };
    auto peek = [&](int line) { return __hip_atomic_load((GAS unsigned*)(p.ws + WS_CNT + line * 256), RLX_AGENT); };
    auto need = [&](unsigned seen, int line, unsigned want) { if ((unsigned)__builtin_amdgcn_readfirstlane(seen) < want) cnt_wait(p.ws, line, want); asm volatile("" ::: "memory"); };
    if (WAIT) bar();
    { u32x4 sv[8]; const __amdgpu_buffer_rsrc_t rs = mkrs(p.ws + WS_ST1, 262144);
      wait_vm<0>(); bar(); UNR for (int i = 0; i < 8; ++i) sv[i] = ld16_dev(rs, so + i * 32768);
      bars(3); *(LAS f32x2*)(smem + L_TB1 + row * 8) = stats_comb<8, 64>(sv); wait_lgkm0(); }
    bars(15);
    { const f32x4 s4 = *(LAS f32x4*)(smem + L_ST + row * 16); const float d = s4[0] - s4[2];
      st8_dev(mkrs(p.ws + WS_ST2, 7 * 2048 * 16), (cb >> 1) * 32768 + so + (cb & 1) * 8, (u32x2){__float_as_uint(0.5f * (s4[0] + s4[2])), __float_as_uint(s4[1] + s4[3] + 32.0f * d * d)}); }
    bar(); wait_vm<0>(); bar();
    if (row == 0) cnt_add(p.ws, C_S2 + rb);
    STAMPS2(12); bar();
    { const unsigned s1 = peek(C_S2 + rb); bar(); STAMPS2(14); need(s1, C_S2 + rb, 14u); STAMPS2(15);
      u32x4 sv[7]; const __amdgpu_buffer_rsrc_t rs = mkrs(p.ws + WS_ST2, 7 * 2048 * 16); UNR for (int i = 0; i < 7; ++i) sv[i] = ld16_dev(rs, so + i * 32768);
      bars(2); const f32x2 st = stats_comb<7, 128>(sv); *(LAS f32x2*)(smem + L_TB2 + row * 8) = (f32x2){st[1], -st[0] * st[1]}; wait_lgkm0(); }
    bar(); STAMPS2(16); bar(); STAMPS2(17);
    bars(3);
    if (row == 0) cnt_add(p.ws, C_Z + cb);
    STAMPS2(18); bars(2);
    STAMPS2(19); cnt_wait(p.ws, C_Z + cb, 16u); STAMPS2(20);
    bars(35);
    UNR for (int i = 0; i < 4; ++i) bs[i] = bf_lo(p.b_sp[rb * 128 + 32 * i + (tid >> 4)]);
  } else if (wave >= 4) {
    const int lw = wave - 4; const char* A1 = p.ws + WS_Y + (size_t)rb * 128 * D_ * 2; const char* A2 = (const char*)p.w_sp + (size_t)rb * 128 * S_ * 2;
    const char* B1 = (const char*)p.w_in + cb * 256; const char* B2 = p.ws + WS_Z2N + cb * 256;
    const unsigned va1 = voff_a(lw, lane, D_), va2 = voff_a(lw, lane, S_), vb1 = voff_b(lw, lane, 3584), vb2 = voff_b(lw, lane, FH);
    const unsigned s0 = (unsigned)(size_t)smem + lw * 1024;
    auto ia = [&](int t, int slot) { const unsigned s = s0 + slot * 32768;
      if (t < 32) { const char* a = A1 + ((t + t0) & 15) * 128; dma4h<true>(s, va1, a, 32 * D_); dma4h<true>(s + 8192, va1, a + 128 * D_, 32 * D_); }
      else { const char* a = A2 + (t - 32) * 128; dma4h<false>(s, va2, a, 32 * S_); dma4h<false>(s + 8192, va2, a + 128 * S_, 32 * S_); } };
    auto ib = [&](int t, int slot) { const unsigned s = s0 + slot * 32768 + 16384;
      if (t < 32) { const char* b = B1 + (size_t)((t + t0) & 15) * 64 * 3584 * 2 + (t < 16 ? FH * 2 : 0); dma4h<false>(s, vb1, b, 32 * 3584); dma4h<false>(s + 8192, vb1, b + 128, 32 * 3584); }
      else { const char* b = B2 + (size_t)(t - 32) * 64 * FH * 2; dma4h<true>(s, vb2, b, 32 * FH); dma4h<true>(s + 8192, vb2, b + 128, 32 * FH); } };
    int slot = 0;
    auto steps = [&](int b0, int b1) { for (int b = b0; b <= b1; ++b) { wait_vm<16>(); bar(); ia(b + 2, slot); ib(b + 2, slot); slot = (slot == 2) ? 0 : slot + 1; } };
    if (!WAIT) { UNR for (int t = 0; t < 3; ++t) ib(t, t); }
    if (WAIT) bar();
    __builtin_amdgcn_s_setprio(3);
    ia(0, 0); ia(1, 1); wait_vm<8>(); bar();
    ia(2, 2); wait_vm<8>(); bar(); ia(3, 0); ib(3, 0); slot = 1;
    STAMPL(1, 8); steps(2, 12); STAMPL(1, 9); steps(13, 16); STAMPL(1, 10); bar(); steps(17, 24);
    bar();
    steps(25, 32); bar(); steps(33, 33); STAMPL(1, 21); steps(34, 61); STAMPL(1, 23);
    wait_vm<16>(); bar(); wait_vm<0>(); bar();
    __builtin_amdgcn_s_setprio(0);
    UNR for (int i = 0; i < 4; ++i) bs[i] = bf_lo(p.b_sp[rb * 128 + 32 * i + (tid >> 4)]);
  } else {
    const unsigned sb = (unsigned)(size_t)smem; const int wm = wave >> 1, wn = wave & 1;
    ConsG c; c.init(sb, sb + L_XT, t0, wm, wn, lane);
    auto frag_at = [&](int base, int mt, int j, int r, int g) { const int row = 64 * wm + 16 * mt + r, ch = 8 * ((j >> 1) ^ wm) + 4 * wn + 2 * (j & 1) + (g >> 1); return (LAS u32x2*)(smem + base + row * 256 + ((ch ^ (row & 15)) * 16) + (g & 1) * 8); };
    auto leave = [&](bool z2) {
      mfma_drain();
      f32x4 cs[4], bi[4]; f32x2 st[4]; const int l2 = fresh(threadIdx.x), r = l2 & 15, g = (l2 >> 4) & 3;
      if (r < 2) { UNR for (int j = 0; j < 2; ++j) *(LAS f32x4*)(smem + L_TCS + r * 512 + (64 * wm + 32 * wn + 16 * j + 4 * g) * 4) = c.xa[j]; }
      wait_lgkm0(); bar();
      UNR for (int j = 0; j < 4; ++j) { LAS char* t = smem + L_TCS + (64 * ((j >> 1) ^ wm) + 32 * wn + 16 * (j & 1) + 4 * g) * 4; cs[j] = *(LAS f32x4*)t; bi[j] = *(LAS f32x4*)(t + 512); }
      UNR for (int mt = 0; mt < 4; ++mt) st[mt] = *(LAS f32x2*)(smem + L_TB1 + (64 * wm + 16 * mt + r) * 8);
      UNR for (int mt = 0; mt < 4; ++mt) { f32x2 z[8], w[8];
        UNR for (int j = 0; j < 4; ++j) { z[2 * j] = lo2(c.acc[mt][j]) - lo2(cs[j]) * st[mt][0]; z[2 * j + 1] = hi2(c.acc[mt][j]) - hi2(cs[j]) * st[mt][0]; }
        SB();
        UNR for (int j = 0; j < 4; ++j) { z[2 * j] = z[2 * j] * st[mt][1] + lo2(bi[j]); z[2 * j + 1] = z[2 * j + 1] * st[mt][1] + hi2(bi[j]); }
        SB();
        { const float k1 = -2.0f * 0.7978845608028654f * 1.4426950408889634f, k3 = k1 * 0.044715f;
          UNR for (int e = 0; e < 8; ++e) w[e] = z[e] * z[e];
          SB(); UNR for (int e = 0; e < 8; ++e) w[e] = w[e] * k3 + k1;
          SB(); UNR for (int e = 0; e < 8; ++e) w[e] *= z[e];
          SB(); UNR for (int e = 0; e < 8; ++e) w[e] = ex2(w[e]);
          SB(); UNR for (int e = 0; e < 8; ++e) w[e] += 1.0f;
          SB(); UNR for (int e = 0; e < 8; ++e) w[e] = (f32x2){__builtin_amdgcn_rcpf(w[e][0]), __builtin_amdgcn_rcpf(w[e][1])};
          SB(); UNR for (int e = 0; e < 8; ++e) { z[e] *= w[e]; pin(z[e]); }
          SB(); }
        if (z2) { const f32x2 t = ((z[0] + z[1]) + (z[2] + z[3])) + ((z[4] + z[5]) + (z[6] + z[7])); float m = t[0] + t[1];
          UNR for (int j = 0; j < 4; ++j) *frag_at(L_IMG, mt, j, r, g) = (u32x2){pk2v(z[2 * j]), pk2v(z[2 * j + 1])};
          m = x16sum(m) * (1.0f / 64.0f); f32x2 q0 = {0.f, 0.f}, q1 = q0;
          UNR for (int e = 0; e < 4; ++e) { const f32x2 d0 = z[2 * e] - m, d1 = z[2 * e + 1] - m; q0 += d0 * d0; q1 += d1 * d1; }
          q0 += q1; const float qs = x16sum(q0[0] + q0[1]);
          if (g == 0) *(LAS f32x2*)(smem + L_ST + (64 * wm + 16 * mt + r) * 16 + wn * 8) = (f32x2){m, qs}; }
        else { UNR for (int j = 0; j < 4; ++j) *frag_at(L_IMG, mt, j, r, g) = (u32x2){pk2v(z[2 * j]), pk2v(z[2 * j + 1])}; } }
      wait_lgkm0(); SB(); };
    if (WAIT) { if (wave == 0) cnt_wait(p.ws, C_Y + rb, 16u); bar(); }
    STAMP(2, 15); STAMPC(15); c.start(); STAMP(2, 16); STAMPC(16); c.template run<true>(16, true); STAMP(2, 17); STAMPC(17);
    leave(true);
    STAMP(2, 19); STAMPC(19); c.template run<true>(8, true);
    bar(); normalize(); SB();
    c.template run<true>(2, false); wait_vm<0>(); c.template run<true>(6, false); STAMP(2, 22); STAMPC(22);
    leave(false);
    STAMP(2, 23); STAMPC(23);
    c.template run<false>(30, true);
    { const int t2 = fresh(threadIdx.x); UNR for (int i = 0; i < 4; ++i) bs[i] = bf_lo(p.b_sp[rb * 128 + 32 * i + (t2 >> 4)]); }
    c.run_last2(); STAMP(2, 24); STAMPC(24);
    const int l2 = fresh(threadIdx.x);
    UNR for (int mt = 0; mt < 4; ++mt) UNR for (int j = 0; j < 4; ++j) *frag_at(L_G, mt, j, l2 & 15, (l2 >> 4) & 3) = (u32x2){pk2(c.acc[mt][j][0], c.acc[mt][j][1]), pk2(c.acc[mt][j][2], c.acc[mt][j][3])};
    wait_lgkm0();
  }
  bar();
  { const __amdgpu_buffer_rsrc_t ro = mkrs(p.ws + WS_U, 8u << 20); const int tid = fresh(threadIdx.x);
    UNR for (int i = 0; i < 4; ++i) { const int row = 32 * i + (tid >> 4), ch = tid & 15; const u32x4 z = *chunk_at(L_IMG, row, ch), gg = *chunk_at(L_G, row, ch); u32x4 o;
      UNR for (int e = 0; e < 4; ++e) o[e] = pk2v(up2(z[e]) * (up2(gg[e]) + bs[i]));
      st16_dev(ro, (unsigned)((rb * 128 + row) * FH + cb * 128 + ch * 8) * 2, o); } }
  STAMP(2, 21); STAMPC(21);
  if (PUB) publish_and_open(p, wave, tid, C_U + rb, true, [&](int h) { UNR for (int t = 4 * h; t < 4 + 2 * h; ++t) proj_ib<true>(p, (unsigned)(size_t)smem, wave & 3, lane, t, t); });
  STAMP(2, 25); STAMPC(25);
}

template <int LO, int HI>
__global__ __launch_bounds__(512, 2) void k_main(Prm p) {
  extern __shared__ __attribute__((aligned(16))) char smem_[];
  LAS char* smem = (LAS char*)smem_;
  const int tid = threadIdx.x, lane = tid & 63, wave = __builtin_amdgcn_readfirstlane(tid >> 6);
  if (wave == 2) {
    unsigned long long pc; unsigned len; asm volatile("s_getpc_b64 %0\n.Lkm_begin:\n\ts_mov_b32 %1, .Lkm_end-.Lkm_begin" : "=s"(pc), "=s"(len));
    const unsigned l = (blockIdx.x >> 3) + 32 * (lane & 31);
    touch_lds((unsigned)(size_t)smem + L_DUMMY, (const char*)pc + (l * 128 < len ? l * 128 : 0));
  }
  Pre pr;
  if (LO <= 1 && HI >= 1) phase_qkv<(HI > 1)>(p, smem, wave, lane, tid, pr);
  if (LO <= 2 && HI >= 2) phase_proj<false, (LO < 2), (HI > 2)>(p, smem, wave, fresh(tid) & 63, fresh(tid), pr);
  if (LO <= 3 && HI >= 3) phase_gmlp<(LO < 3), (HI > 3)>(p, smem, wave, fresh(tid) & 63, fresh(tid));
  if (LO <= 4 && HI >= 4) phase_proj<true, (LO < 4), false>(p, smem, wave, fresh(tid) & 63, fresh(tid), pr);
  asm volatile(".Lkm_end:");
}

#define FUSE 1
template <int LO, int HI> static void launch_main(const Prm& p, hipStream_t stream) {
  (void)hipFuncSetAttribute((const void*)k_main<LO, HI>, hipFuncAttributeMaxDynamicSharedMemorySize, 163840);
  k_main<LO, HI><<<256, 512, 163840, stream>>>(p);
}
extern "C" void kernel_launch(void* const* d_in, const int* in_sizes, int n_in, void* d_out, int out_size, void* d_ws, size_t ws_size, hipStream_t stream) {
  Prm p;
  p.x = (const u16*)d_in[0]; p.wqkv = (const u16*)d_in[1]; p.wphi = (const u16*)d_in[2]; p.wo = (const u16*)d_in[3];
  p.w_in = (const u16*)d_in[5]; p.w_sp = (const u16*)d_in[6]; p.b_sp = (const u16*)d_in[7]; p.w_out = (const u16*)d_in[8];
  p.c = (const u16*)d_in[9]; p.w_mod = (const u16*)d_in[10]; p.out = (u16*)d_out; p.ws = (char*)d_ws;
  k_mod<<<256, 256, 0, stream>>>(p);
  launch_main<1, 4>(p, stream);
}
```

```cpp
#include <hip/hip_runtime.h>
#include <stdint.h>

typedef unsigned short u16;
typedef __bf16 bf16x8 __attribute__((ext_vector_type(8)));
typedef __bf16 bf16x2 __attribute__((ext_vector_type(2)));
typedef short s16x4 __attribute__((ext_vector_type(4)));
typedef float f32x2 __attribute__((ext_vector_type(2)));
typedef float f32x4 __attribute__((ext_vector_type(4)));
typedef unsigned u32x4 __attribute__((ext_vector_type(4)));
typedef unsigned u32x2 __attribute__((ext_vector_type(2)));
#define LAS __attribute__((address_space(3)))
#define GAS __attribute__((address_space(1)))
#define DI __device__ __forceinline__
#define SB() __builtin_amdgcn_sched_barrier(0)
#define RLX_AGENT __ATOMIC_RELAXED, __HIP_MEMORY_SCOPE_AGENT
#define UNR _Pragma("unroll")

constexpr int S_ = 2048, D_ = 1024, FH = 1792;
constexpr size_t WS_FLG  = 0;
constexpr size_t WS_MODA = 65536;
constexpr size_t WS_MODB = WS_MODA + 131072;
constexpr size_t WS_MODF = WS_MODB + 65536;
constexpr size_t WS_CSB  = WS_MODF + 16384;
constexpr size_t WS_ZP   = WS_CSB + 65536;
constexpr size_t WS_ST1  = WS_ZP + 131072;
constexpr size_t WS_ST2  = WS_ST1 + 262144;
constexpr size_t WS_KVP  = 1u << 20;
constexpr size_t WS_H    = WS_KVP + (2u << 20);
constexpr size_t WS_O    = WS_H + (4u << 20);
constexpr size_t WS_Y    = WS_O + (4u << 20);
constexpr size_t WS_Z2N  = WS_Y + (4u << 20);
constexpr size_t WS_U    = WS_Z2N + (8u << 20);
static_assert(WS_ST2 + 7 * 2048 * 16 <= WS_KVP, "ws map");
constexpr int F_KV = 0, F_O = 16, F_Y = 32, F_S2 = 48, F_Z = 64, F_U = 80, F_H = 96, F_EPOCH = 126, F_TMO = 127, F_CS = 128;
constexpr unsigned SPIN_CAP = 1u << 20;

#define STAMPS 0
constexpr size_t WS_STAMP = 64u << 20;
#define STAMP(k, i) do {} while (0)
#define STAMPL(k, i) do {} while (0)
#define STAMPC(i) do {} while (0)
#define STAMPS2(i) do {} while (0)
#define HINT_SLEEP 4
#define RETRY_SLEEP 8
#define ZROT 0
#define PROT2 0
#define PROT4 0
#define PACE 20
#define CODEPF 1
struct Args { const u16 *x, *wqkv, *wphi, *wo, *w_in, *w_sp, *b_sp, *w_out, *c, *w_mod; u16* out; char* ws; };
struct Prm : Args { unsigned long long tag; };

template <int N> DI void wait_vm() { asm volatile("s_waitcnt vmcnt(%0)" ::"n"(N) : "memory"); }
DI void wait_lgkm0() { __builtin_amdgcn_s_waitcnt(0xC07F); }
DI void bar() { asm volatile("" ::: "memory"); __builtin_amdgcn_s_barrier(); asm volatile("" ::: "memory"); }
#define DMA_OP(sfx, n, off) "s_add_i32 m0, %1, " off "\n\ts_nop 0\n\tglobal_load_lds_dwordx4 %2, %" n sfx "\n\t"
#define DMA4_STR(sfx) "s_nop 4\n\ts_mov_b32 %0, m0\n\t" DMA_OP(sfx, "3", "0") DMA_OP(sfx, "4", "0x1000") DMA_OP(sfx, "5", "0x2000") DMA_OP(sfx, "6", "0x3000") "s_mov_b32 m0, %0"
#define DMA2_STR(sfx) "s_nop 4\n\ts_mov_b32 %0, m0\n\t" DMA_OP(sfx, "3", "0") DMA_OP(sfx, "4", "0x1000") "s_mov_b32 m0, %0"
template <bool DEV> DI void dma4(unsigned lds, unsigned voff, const char* b0, const char* b1, const char* b2, const char* b3) { unsigned keep;
  if (DEV) asm volatile(DMA4_STR(" sc1") : "=&s"(keep) : "s"(lds), "v"(voff), "s"(b0), "s"(b1), "s"(b2), "s"(b3) : "memory", "scc");
  else asm volatile(DMA4_STR("") : "=&s"(keep) : "s"(lds), "v"(voff), "s"(b0), "s"(b1), "s"(b2), "s"(b3) : "memory", "scc"); }
#define DMA4H_STR(sfx) "s_nop 4\n\ts_mov_b32 %0, m0\n\t" DMA_OP(sfx, "3", "0") DMA_OP(sfx, "4", "0x800") DMA_OP(sfx, "5", "0x1000") DMA_OP(sfx, "6", "0x1800") "s_mov_b32 m0, %0"
template <bool DEV> DI void dma4h(unsigned lds, unsigned voff, const char* b, size_t d) { unsigned keep;
  if (DEV) asm volatile(DMA4H_STR(" sc1") : "=&s"(keep) : "s"(lds), "v"(voff), "s"(b), "s"(b + d), "s"(b + 2 * d), "s"(b + 3 * d) : "memory", "scc");
  else asm volatile(DMA4H_STR("") : "=&s"(keep) : "s"(lds), "v"(voff), "s"(b), "s"(b + d), "s"(b + 2 * d), "s"(b + 3 * d) : "memory", "scc"); }
template <bool DEV> DI void dma2(unsigned lds, unsigned voff, const char* b0, const char* b1) { unsigned keep;
  if (DEV) asm volatile(DMA2_STR(" sc1") : "=&s"(keep) : "s"(lds), "v"(voff), "s"(b0), "s"(b1) : "memory", "scc");
  else asm volatile(DMA2_STR("") : "=&s"(keep) : "s"(lds), "v"(voff), "s"(b0), "s"(b1) : "memory", "scc"); }
DI void dma1(unsigned lds, unsigned voff, const char* b0) { unsigned keep;
  asm volatile("s_nop 4\n\ts_mov_b32 %0, m0\n\ts_mov_b32 m0, %1\n\ts_nop 0\n\tglobal_load_lds_dwordx4 %2, %3\n\ts_mov_b32 m0, %0" : "=&s"(keep) : "s"(lds), "v"(voff), "s"(b0) : "memory"); }
DI void touch_lds(unsigned lds, const char* p) { unsigned keep;
  asm volatile("s_mov_b32 %0, m0\n\ts_mov_b32 m0, %1\n\ts_nop 0\n\tglobal_load_lds_dword %2, off\n\ts_mov_b32 m0, %0" : "=&s"(keep) : "s"(lds), "v"(p) : "memory"); }
DI void touch_code(unsigned lds, int lane) { unsigned long long pc; unsigned back, len;
  asm volatile("s_getpc_b64 %0\n1:\n\ts_mov_b32 %1, 1b-.Lkm_begin\n\ts_mov_b32 %2, .Lkm_end-.Lkm_begin" : "=s"(pc), "=s"(back), "=s"(len));
  const unsigned l = ((blockIdx.x >> 3) + 32 * (lane & 31)) * 128; touch_lds(lds, (const char*)(pc - back) + (l < len ? l : 0)); }
constexpr int L_DUMMY = 162048, L_X1 = 147456;
DI unsigned pk2(float a, float b) { f32x2 v = {a, b}; return __builtin_bit_cast(unsigned, __builtin_convertvector(v, bf16x2)); }
DI float bf_lo(unsigned u) { return __uint_as_float(u << 16); }
DI float bf_hi(unsigned u) { return __uint_as_float(u & 0xffff0000u); }
DI float rbf(float a) { return bf_lo(pk2(a, a)); }
DI bf16x8 pack8(const f32x4& a, const f32x4& b) { u32x4 r = {pk2(a[0], a[1]), pk2(a[2], a[3]), pk2(b[0], b[1]), pk2(b[2], b[3])}; return __builtin_bit_cast(bf16x8, r); }
DI f32x4 mfma16(bf16x8 a, bf16x8 b, f32x4 c) { return __builtin_amdgcn_mfma_f32_16x16x32_bf16(a, b, c, 0, 0, 0); }
DI void mfma_acc(f32x4& c, bf16x8 a, bf16x8 b, bool first) {
  if (first) asm volatile("v_mfma_f32_16x16x32_bf16 %0, %1, %2, 0" : "=&v"(c) : "v"(a), "v"(b));
  else asm volatile("v_mfma_f32_16x16x32_bf16 %0, %1, %2, %0" : "+v"(c) : "v"(a), "v"(b)); }
DI void mfma_drain() { SB(); asm volatile("s_nop 7\n\ts_nop 7"); SB(); }
DI s16x4 ldtr(unsigned a) { return __builtin_amdgcn_ds_read_tr16_b64_v4i16((LAS s16x4*)(size_t)a); }
DI bf16x8 cat8(s16x4 lo, s16x4 hi) { return __builtin_bit_cast(bf16x8, __builtin_shufflevector(lo, hi, 0, 1, 2, 3, 4, 5, 6, 7)); }
template <int CTRL> DI float dpp(float x) { return __builtin_bit_cast(float, __builtin_amdgcn_mov_dpp(__builtin_bit_cast(int, x), CTRL, 0xf, 0xf, true)); }
DI float x16max(float x) { auto s = __builtin_amdgcn_permlane16_swap(__float_as_uint(x), __float_as_uint(x), false, false); x = fmaxf(__uint_as_float(s[0]), __uint_as_float(s[1]));
  auto t = __builtin_amdgcn_permlane32_swap(__float_as_uint(x), __float_as_uint(x), false, false); return fmaxf(__uint_as_float(t[0]), __uint_as_float(t[1])); }
DI float x16min(float x) { auto s = __builtin_amdgcn_permlane16_swap(__float_as_uint(x), __float_as_uint(x), false, false); x = fminf(__uint_as_float(s[0]), __uint_as_float(s[1]));
  auto t = __builtin_amdgcn_permlane32_swap(__float_as_uint(x), __float_as_uint(x), false, false); return fminf(__uint_as_float(t[0]), __uint_as_float(t[1])); }
DI float x16sum(float x) { auto s = __builtin_amdgcn_permlane16_swap(__float_as_uint(x), __float_as_uint(x), false, false); x = __uint_as_float(s[0]) + __uint_as_float(s[1]);
  auto t = __builtin_amdgcn_permlane32_swap(__float_as_uint(x), __float_as_uint(x), false, false); return __uint_as_float(t[0]) + __uint_as_float(t[1]); }
DI float sum8(float x) { x += dpp<0xB1>(x); x += dpp<0x4E>(x); x += dpp<0x141>(x); return x; }
DI float sum64(float x) { x = sum8(x); x += dpp<0x128>(x); return x16sum(x); }
DI __amdgpu_buffer_rsrc_t mkrs(const void* p, unsigned bytes) { return __builtin_amdgcn_make_buffer_rsrc((void*)p, 0, bytes, 0x00020000); }
DI void st16_dev(__amdgpu_buffer_rsrc_t r, unsigned off, u32x4 v) { __builtin_amdgcn_raw_buffer_store_b128(v, r, off, 0, 16); }
DI void st4_dev(__amdgpu_buffer_rsrc_t r, unsigned off, unsigned v) { __builtin_amdgcn_raw_buffer_store_b32(v, r, off, 0, 16); }
DI void st8_dev(__amdgpu_buffer_rsrc_t r, unsigned off, u32x2 v) { __builtin_amdgcn_raw_buffer_store_b64(v, r, off, 0, 16); }
DI u32x4 ld16_dev(__amdgpu_buffer_rsrc_t r, unsigned off) { return __builtin_amdgcn_raw_buffer_load_b128(r, off, 0, 16); }
DI unsigned ld4_dev(__amdgpu_buffer_rsrc_t r, unsigned off) { return __builtin_amdgcn_raw_buffer_load_b32(r, off, 0, 16); }
typedef GAS unsigned long long gu64;
DI gu64* flag_at(const Prm& p, int line) { return (gu64*)(p.ws + WS_FLG + line * 256); }
DI void post(const Prm& p, int line, int slot) { __hip_atomic_store(flag_at(p, line) + slot, p.tag, RLX_AGENT); }
DI unsigned long long peek(const Prm& p, int line, int n, int lane) { return __hip_atomic_load(flag_at(p, line) + (lane < n ? lane : 0), RLX_AGENT); }
DI bool give_up(const Prm& p, unsigned& spins) {
  __builtin_amdgcn_s_sleep(1);
  if (++spins > SPIN_CAP) { __hip_atomic_store(flag_at(p, F_TMO), p.tag, RLX_AGENT); return true; }
  return (spins & 255u) == 0u && __all(__hip_atomic_load(flag_at(p, F_TMO), RLX_AGENT) == p.tag);
}
DI void await(const Prm& p, int line, int n, int lane) {
  for (unsigned spins = 0;;) { if (__all(peek(p, line, n, lane) == p.tag) || give_up(p, spins)) break; }
  asm volatile("" ::: "memory");
}
DI void swap16(float& a, float& b) { auto s = __builtin_amdgcn_permlane16_swap(__float_as_uint(a), __float_as_uint(b), false, false); a = __uint_as_float(s[0]); b = __uint_as_float(s[1]); }
DI void swap32(float& a, float& b) { auto s = __builtin_amdgcn_permlane32_swap(__float_as_uint(a), __float_as_uint(b), false, false); a = __uint_as_float(s[0]); b = __uint_as_float(s[1]); }
DI f32x2 lo2(const f32x4& v) { return __builtin_shufflevector(v, v, 0, 1); }
DI f32x2 hi2(const f32x4& v) { return __builtin_shufflevector(v, v, 2, 3); }
DI f32x2 ex2(f32x2 v) { return (f32x2){__builtin_amdgcn_exp2f(v[0]), __builtin_amdgcn_exp2f(v[1])}; }
DI f32x2 up2(unsigned u) { return (f32x2){bf_lo(u), bf_hi(u)}; }
DI unsigned pk2v(f32x2 v) { return __builtin_bit_cast(unsigned, __builtin_convertvector(v, bf16x2)); }
DI f32x2 gelu2(f32x2 x) {
  const float k1 = -2.0f * 0.7978845608028654f * 1.4426950408889634f, k3 = k1 * 0.044715f;
  const f32x2 d = ex2(x * ((x * x) * k3 + k1)) + 1.0f;
  return x * (f32x2){__builtin_amdgcn_rcpf(d[0]), __builtin_amdgcn_rcpf(d[1])};
}
DI int fresh(int v) { asm volatile("" : "+v"(v)); return v; }
template <class T> DI void pinv(T& v) { asm volatile("" : "+v"(v)); }
template <class T> DI void pin(T& v) { asm volatile("" : "+v"(v) :: "memory"); }
struct LdAddr { unsigned a, b; };
DI unsigned voff_a(int lw, int lane, int lda) { const int r = 8 * lw + (lane >> 3); return (unsigned)(r * lda * 2 + (((lane & 7) ^ ((r >> 1) & 7)) * 16)); }
DI unsigned voff_b(int lw, int lane, int ldb) { const int rip = lane >> 3, f = ((rip >> 1) & 1) | ((lw & 1) << 1); return (unsigned)((8 * lw + rip) * ldb * 2 + (((lane & 7) ^ (f << 1)) * 16)); }
template <bool DEV> DI void issue_a(unsigned s, const char* ga, int lda, unsigned va) { const size_t d = (size_t)64 * lda; dma4<DEV>(s, va, ga, ga + d, ga + 2 * d, ga + 3 * d); }
template <int NP, bool DEV> DI void issue_b(unsigned s, const char* gb, int ldb, int pstride, unsigned vb) { const size_t d = (size_t)64 * ldb, e = (size_t)2 * pstride;
  if (NP == 1) dma2<DEV>(s + 16384, vb, gb, gb + d);
  else dma4<DEV>(s + 16384, vb, gb, gb + d, gb + e, gb + e + d);
  if (NP == 3) dma2<DEV>(s + 32768, vb, gb + 2 * e, gb + 2 * e + d); }
template <int NST, int PB, int KT, int LAST, bool ALLB, class FA, class FB>
DI void loader_run(FA ia, FB ib) {
  constexpr int P = 4 + PB;
  if (!ALLB) {
    ia(0, 0); ib(1, 1); ia(1, 1); wait_vm<P>(); bar();
    UNR for (int t = 2; t < NST; ++t) { ib(t, t); ia(t, t); }
    int slot = 0;
    for (int b = 1; b <= KT - NST; ++b) { wait_vm<(NST - 2) * P>(); bar(); ia(b - 1 + NST, slot); ib(b - 1 + NST, slot); slot = (slot + 1 == NST) ? 0 : slot + 1; }
    static_assert(ALLB || LAST == KT - NST, "returns after the last issue");
    return;
  }
  ia(0, 0); ia(1, 1);
  wait_vm<4>(); bar();
  UNR for (int t = 2; t < NST; ++t) ia(t, t);
  UNR for (int b = 1; b < NST; ++b) {
    if (b == 1) { wait_vm<(NST - 2) * 4>(); }
    else if (b == 2) { wait_vm<(NST > 2 ? (NST - 3) * 4 + P : 0)>(); }
    else if (b == 3) { wait_vm<(NST > 3 ? (NST - 4) * 4 + 2 * P : 0)>(); }
    else if (b == 4) { wait_vm<(NST > 4 ? (NST - 5) * 4 + 3 * P : 0)>(); }
    else { wait_vm<(NST > 5 ? (NST - 6) * 4 + 4 * P : 0)>(); }
    bar(); ia(b - 1 + NST, b - 1); ib(b - 1 + NST, b - 1); }
  int slot = NST - 1;
  for (int b = NST; b <= KT - NST; ++b) { wait_vm<(NST - 2) * P>(); bar(); ia(b - 1 + NST, slot); ib(b - 1 + NST, slot); slot = (slot + 1 == NST) ? 0 : slot + 1; }
  UNR for (int b = KT - NST + 1; b <= LAST; ++b) { const int al = KT - 1 - b;
    if (al >= 4) wait_vm<4 * P>(); else if (al == 3) wait_vm<3 * P>(); else if (al == 2) wait_vm<2 * P>(); else if (al == 1) wait_vm<P>(); else wait_vm<0>();
    bar(); }
}

template <int NP, int KS, int NST, int NSWAP>
struct Cons {
  static constexpr int MT = 4, NT = 2 * NP, STG = 16384 + NP * 8192, NR1 = MT + 2 * NT, NM1 = MT * NT, NR = NR1 * KS, NM = NM1 * KS, NADV = 4;
  static constexpr int RPS = (NR + NADV <= NM) ? 1 : 2;
  f32x4 acc[MT][NT]; bf16x8 fa[2][KS][MT]; s16x4 fb[2][KS][NT][2]; unsigned ad[4]; int slot;
  DI void init(unsigned sb, int wm, int wn, int lane) {
    const int r = lane & 15, g = lane >> 4, q = r >> 2, p4 = lane & 3, f = ((q >> 1) & 1) | ((g & 1) << 1), arow = wm * 64 + r;
    ad[0] = sb + arow * 128 + ((g ^ ((r >> 1) & 7)) * 16); ad[1] = sb + arow * 128 + (((4 + g) ^ ((r >> 1) & 7)) * 16);
    ad[2] = sb + 16384 + (8 * g + q) * 128 + (((2 * wn) ^ f) * 32) + p4 * 8; ad[3] = sb + 16384 + (8 * g + q) * 128 + (((2 * wn + 1) ^ f) * 32) + p4 * 8;
    slot = 0;
  }
  DI void rd1(int buf, int kb, int ks, int n) {
    if (n == 0) { fa[buf][kb][0] = *(LAS bf16x8*)(size_t)(ad[ks]); return; }
    if (n > 2 * NT) { const int i = n - 2 * NT; fa[buf][kb][i] = *(LAS bf16x8*)(size_t)(ad[ks] + i * 2048); return; }
    const int j = (n - 1) >> 1, hf = (n - 1) & 1;
    fb[buf][kb][j][hf] = ldtr(ad[2 + (j & 1)] + (j >> 1) * 8192 + ks * 4096 + hf * 512);
  }
  DI void mm1(int buf, int kb, int n, bool first) {
    const int i = n / NT, j = n % NT; const bf16x8 b = cat8(fb[buf][kb][j][0], fb[buf][kb][j][1]);
    if (j >= NSWAP) mfma_acc(acc[i][j], fa[buf][kb][i], b, first); else mfma_acc(acc[i][j], b, fa[buf][kb][i], first);
  }
  DI int next_delta() { const bool w = (slot + 1 == NST); slot = w ? 0 : slot + 1; return w ? -(NST - 1) * STG : STG; }
  DI void phase(int use, int ks0, bool rd, bool adv, int delta, bool first = false) {
    UNR for (int n = 0; n < NM; ++n) {
      mm1(use, n / NM1, n % NM1, first && n < NM1); SB();
      UNR for (int u = 0; u < RPS; ++u) { const int x = n * RPS + u;
        if (rd && x < NR) rd1(use ^ 1, x / NR1, ks0 + x / NR1, x % NR1);
        if (adv && x >= NR && x < NR + NADV) { ad[x - NR] += delta; asm volatile("" : "+v"(ad[x - NR])); } }
      SB();
    }
  }
  DI void start() {
    bar();
    UNR for (int n = 0; n < NR; ++n) rd1(0, n / NR1, n / NR1, n % NR1);
    if (KS == 2) { const int d = next_delta(); UNR for (int n = 0; n < NADV; ++n) ad[n] += d; }
    wait_lgkm0(); SB();
  }
  DI void run_half(int kt) {
    { const int d = next_delta(); phase(0, 1, true, true, d, true); wait_lgkm0(); bar(); SB(); phase(1, 0, true, false, 0); wait_lgkm0(); SB(); }
    for (int t = 1; t < kt - 1; ++t) { const int d = next_delta(); phase(0, 1, true, true, d); wait_lgkm0(); bar(); SB(); phase(1, 0, true, false, 0); wait_lgkm0(); SB(); }
    phase(0, 1, true, false, 0); wait_lgkm0(); bar(); SB(); phase(1, 0, false, false, 0); mfma_drain();
  }
  DI void run_first2() {
    bar(); SB(); { const int d = next_delta(); phase(0, 0, true, true, d, true); } wait_lgkm0();
    bar(); SB(); { const int d = next_delta(); phase(1, 0, true, true, d); } wait_lgkm0(); SB();
  }
  DI void run_pairs(int n) {
    for (int t = 0; t < n; t += 2) {
      bar(); SB(); { const int d = next_delta(); phase(0, 0, true, true, d); } wait_lgkm0();
      bar(); SB(); { const int d = next_delta(); phase(1, 0, true, true, d); } wait_lgkm0(); SB();
    }
  }
  DI void run_last2() { bar(); SB(); phase(0, 0, true, false, 0); wait_lgkm0(); SB(); phase(1, 0, false, false, 0); mfma_drain(); }
};

template <int NP2, int NC> DI f32x2 stats_comb(u32x4 (&v)[NP2]) {
  float m = 0.f; UNR for (int i = 0; i < NP2; ++i) m += __uint_as_float(v[i][0]) + __uint_as_float(v[i][2]);
  m *= 1.0f / (2 * NP2); float q = 0.f;
  UNR for (int i = 0; i < NP2; ++i) { const float d0 = __uint_as_float(v[i][0]) - m, d1 = __uint_as_float(v[i][2]) - m; q += (__uint_as_float(v[i][1]) + __uint_as_float(v[i][3])) + (float)NC * (d0 * d0 + d1 * d1); }
  return (f32x2){m, __builtin_amdgcn_rsqf(q * (1.0f / (2 * NC * NP2)) + 1e-5f)};
}

struct Pre { u32x4 xv[2]; };
constexpr int L_MT2 = 163328, L_MT4 = 139264;
DI void pre_load(const Prm& p, int tid, Pre& q) {
  const int rb = ((blockIdx.x & 7) >> 1) * 4 + (blockIdx.x >> 6), col = ((blockIdx.x & 1) * 8 + ((blockIdx.x >> 3) & 7)) * 64 + (tid & 7) * 8;
  UNR for (int i = 0; i < 2; ++i) q.xv[i] = __builtin_nontemporal_load((const u32x4*)(p.x + (size_t)(rb * 128 + (tid >> 3) + 64 * i) * D_ + col));
}
struct Tab { u32x4 a, b; };
DI Tab tab_load(const Prm& p, int lane, int o0, int o1) { const int col = ((blockIdx.x & 1) * 8 + ((blockIdx.x >> 3) & 7)) * 64; const __amdgpu_buffer_rsrc_t r = mkrs(p.ws + WS_MODB, 65536);
  const unsigned o = (unsigned)((lane >> 5) * 4096 + ((lane & 16) ? o1 : o0) + col + (lane & 15) * 4) * 4; return Tab{ld16_dev(r, o), ld16_dev(r, o + 32768)}; }
DI void tab_store(LAS char* t, int lane, const Tab& q) { f32x4 v = __builtin_bit_cast(f32x4, q.a) + __builtin_bit_cast(f32x4, q.b);
  UNR for (int j = 0; j < 4; ++j) { float a = v[j], b = a; swap32(a, b); a = rbf(a + b); v[j] = (lane & 16) ? rbf(1.0f + a) : a; }
  if (lane < 32) *(LAS f32x4*)(t + lane * 16) = v;
}
template <bool LAST> DI void proj_ib(const Prm& p, unsigned sb, int lw, int lane, int t, int slot) {
  constexpr int KT = LAST ? 28 : 16; const int idx = blockIdx.x >> 3, cb = (blockIdx.x & 1) * 8 + (idx & 7); t += (LAST ? PROT4 : PROT2) * (idx >> 3) * (KT / 4); if (t >= KT) t -= KT;
  issue_b<1, false>(sb + slot * 24576 + lw * 1024, (const char*)(LAST ? p.w_out : p.wo) + cb * 128 + (size_t)t * 64 * D_ * 2, D_, 64, voff_b(lw, lane, D_));
}
DI void gmlp_ib0(const Prm& p, unsigned sb, int lw, int lane, int t) {
  const int idx = blockIdx.x >> 3, cb = (blockIdx.x & 1) * 7 + idx % 7;
  issue_b<2, false>(sb + t * 32768 + lw * 1024, (const char*)p.w_in + cb * 256 + (size_t)((t + ZROT * 4 * (idx / 7)) & 15) * 64 * 3584 * 2 + FH * 2, 3584, 64, voff_b(lw, lane, 3584));
}
template <class F> DI void publish_and_open(const Prm& p, int wave, int tid, int line, int slot, bool open, F issue) {
  bar();
  if (wave >= 4 && open) { issue(0); wait_vm<8>(); } else wait_vm<0>();
  bar();
  if (tid == 0 && line >= 0) post(p, line, slot);
  if (wave >= 4 && open) issue(1);
}
DI void softmax_pm2(const f32x4 (&p)[2][2], u32x2 (&o)[2][4]) {
  const float L2E = 1.4426950408889634f; float m[2], m2[2]; f32x2 a[2][4], b[2][4];
  UNR for (int u = 0; u < 2; ++u) { const f32x4 &p0 = p[u][0], &p1 = p[u][1];
    m[u] = fmaxf(fmaxf(fmaxf(fmaxf(p0[0], p0[1]), p0[2]), fmaxf(fmaxf(p0[3], p1[0]), p1[1])), fmaxf(p1[2], p1[3]));
    m2[u] = -fminf(fminf(fminf(fminf(p0[0], p0[1]), p0[2]), fminf(fminf(p0[3], p1[0]), p1[1])), fminf(p1[2], p1[3])); }
  UNR for (int u = 0; u < 2; ++u) { swap16(m[u], m2[u]); m[u] = fmaxf(m[u], m2[u]); m2[u] = m[u]; }
  UNR for (int u = 0; u < 2; ++u) { swap32(m[u], m2[u]); m[u] = fmaxf(m[u], m2[u]) * -L2E; m2[u] = m[u]; }
  UNR for (int u = 0; u < 2; ++u) swap16(m[u], m2[u]);
  SB();
  UNR for (int u = 0; u < 2; ++u) UNR for (int i = 0; i < 4; ++i) { const f32x2 v = (i & 1) ? hi2(p[u][i >> 1]) : lo2(p[u][i >> 1]); a[u][i] = v * L2E + m[u]; b[u][i] = v * -L2E + m2[u]; }
  SB();
  UNR for (int u = 0; u < 2; ++u) UNR for (int i = 0; i < 4; ++i) { a[u][i] = ex2(a[u][i]); b[u][i] = ex2(b[u][i]); }
  SB();
  UNR for (int u = 0; u < 2; ++u) { const f32x2 sa = (a[u][0] + a[u][1]) + (a[u][2] + a[u][3]), sb = (b[u][0] + b[u][1]) + (b[u][2] + b[u][3]); m[u] = sa[0] + sa[1]; m2[u] = sb[0] + sb[1]; }
  UNR for (int u = 0; u < 2; ++u) { swap16(m[u], m2[u]); m[u] += m2[u]; m2[u] = m[u]; }
  UNR for (int u = 0; u < 2; ++u) { swap32(m[u], m2[u]); m[u] = __builtin_amdgcn_rcpf(m[u] + m2[u]); m2[u] = m[u]; }
  UNR for (int u = 0; u < 2; ++u) swap16(m[u], m2[u]);
  SB();
  UNR for (int u = 0; u < 2; ++u) UNR for (int i = 0; i < 4; ++i) { a[u][i] *= m[u]; b[u][i] *= m2[u]; }
  UNR for (int u = 0; u < 2; ++u) UNR for (int i = 0; i < 2; ++i) { o[u][i] = (u32x2){pk2v(a[u][2 * i]), pk2v(a[u][2 * i + 1])}; o[u][2 + i] = (u32x2){pk2v(b[u][2 * i]), pk2v(b[u][2 * i + 1])}; }
}
DI bf16x8 cat8u(u32x2 a, u32x2 b) { return __builtin_bit_cast(bf16x8, (u32x4){a[0], a[1], b[0], b[1]}); }
DI void phase_qkv(Prm& p, LAS char* smem, int wave, int lane, int tid, Pre& pr) {
  constexpr int NST = 3, KT = 16, L_PHI = 0, L_XKV = 34816, L_OST = 65536, L_WPHI = 122880, L_HX = 126976, L_XZ = 151552, L_KVS = 153600, L_ZS = 161792, L_TAB = 126976;
  const int xcd = blockIdx.x & 7, idx = blockIdx.x >> 3, rb = (xcd >> 2) * 8 + (idx >> 2), hp = (xcd & 3) * 4 + (idx & 3), t0 = 2 * (idx >> 2);
  const unsigned sb = (unsigned)(size_t)smem; const int r = lane & 15, g = lane >> 4, wm = (wave >> 1) & 1, wn = wave & 1;
  const int lw = wave & 3; const char* B = (const char*)p.wqkv + hp * 128; const unsigned vb = voff_b(lw, lane, 3072);
  auto ib = [&](int t, int slot) { issue_b<3, false>(sb + slot * 40960 + lw * 1024, B + (size_t)((t + t0) & 15) * 64 * 3072 * 2, 3072, 1024, vb); };
  STAMP(1, 0);
  auto silu = [&](u16 v) { const float c = bf_lo(v); return rbf(c * __builtin_amdgcn_rcpf(1.0f + __expf(-c))); };
  const int cbb = (blockIdx.x & 63) * 64, kb = (blockIdx.x >> 6) * 256 + ((tid & 255) >> 3); float sl2[8];
  auto fold = [&](const u32x4* w, const float* sl, int n, LAS char* red) { f32x2 a[4];
    UNR for (int i = 0; i < n; ++i) {
      UNR for (int j = 0; j < 4; ++j) { const f32x2 t = up2(w[i][j]) * sl[i]; if (i == 0) a[j] = t; else a[j] += t; } }
    UNR for (int j = 0; j < 4; ++j) UNR for (int e = 0; e < 2; ++e) { float t = a[j][e]; t += dpp<0x128>(t); a[j][e] = x16sum(t); }
    if (lane < 8) { UNR for (int j = 0; j < 4; ++j) *(LAS f32x2*)(red + (lane * 8 + 2 * j) * 4) = a[j]; }
    wait_lgkm0(); };
  {
    const int row = rb * 128 + hp * 8 + wave, b = blockIdx.x, ca = (b & 31) * 64, ka = (b >> 5) * 128 + (tid >> 3); u32x4 xv[2], w[2]; u16 cv[2];
    unsigned long long ep = __hip_atomic_load(flag_at(p, F_EPOCH), RLX_AGENT);
    UNR for (int i = 0; i < 2; ++i) { cv[i] = p.c[ka + 64 * i]; w[i] = __builtin_nontemporal_load((const u32x4*)(p.w_mod + (size_t)(ka + 64 * i) * 6144 + ca + (tid & 7) * 8)); }
    UNR for (int i = 0; i < 2; ++i) xv[i] = __builtin_nontemporal_load((const u32x4*)(p.x + (size_t)row * D_ + i * 512 + lane * 8));
    pin(ep); ++ep;
    p.tag = ((unsigned long long)(unsigned)__builtin_amdgcn_readfirstlane((unsigned)(ep >> 32)) << 32) | (unsigned)__builtin_amdgcn_readfirstlane((unsigned)ep);
    { const float sl[2] = {silu(cv[0]), silu(cv[1])}; fold(w, sl, 2, smem + wave * 256); } bar();
    if (wave >= 4) { UNR for (int i = 0; i < 8; ++i) { sl2[i] = silu(p.c[kb + 32 * i]); pin(sl2[i]); } }
    gu64* gran = (gu64*)(p.ws + WS_MODF);
    if (wave == 1) { gu64* part = (gu64*)(p.ws + WS_MODA) + ca + lane;
      { float t = 0.f; UNR for (int w2 = 0; w2 < 8; ++w2) t += *(LAS float*)(smem + (w2 * 64 + lane) * 4);
        __hip_atomic_store(part + (b >> 5) * 2048, (p.tag << 24) | ((__float_as_uint(t) + 0x80u) >> 8), RLX_AGENT); }
      if (b < 32) {
        unsigned long long pa[8];
        for (unsigned spins = 0;;) { bool ok = true;
          UNR for (int k = 0; k < 8; ++k) { pa[k] = __hip_atomic_load(part + k * 2048, RLX_AGENT); ok &= (pa[k] >> 24) == (p.tag & 0xffffffffffull); }
          if (__all(ok) || give_up(p, spins)) break; }
        float t = 0.f; UNR for (int k = 0; k < 8; ++k) t += __uint_as_float((unsigned)pa[k] << 8);
        if (b >= 16) t = 1.0f + rbf(t);
        __hip_atomic_store(gran + ca + lane, (p.tag << 16) | (pk2(t, t) & 0xffffu), RLX_AGENT); } }
    STAMP(0, 1);
    f32x2 v[8]; UNR for (int j = 0; j < 8; ++j) v[j] = up2(xv[j >> 2][j & 3]);
    const f32x2 t = ((v[0] + v[1]) + (v[2] + v[3])) + ((v[4] + v[5]) + (v[6] + v[7])); const float mu = sum64(t[0] + t[1]) * (1.0f / 1024.0f); f32x2 q2 = {0.f, 0.f};
    UNR for (int j = 0; j < 8; ++j) { v[j] -= mu; q2 += v[j] * v[j]; }
    float rs = __builtin_amdgcn_rsqf(sum64(q2[0] + q2[1]) * (1.0f / 1024.0f) + 1e-5f); pin(rs);
    STAMP(0, 6);
    __builtin_amdgcn_s_sleep(HINT_SLEEP);
    STAMP(0, 7);
    { unsigned long long gv[4];
      for (unsigned spins = 0;;) { bool ok = true;
        UNR for (int k = 0; k < 4; ++k) { gv[k] = __hip_atomic_load(gran + k * 512 + tid, RLX_AGENT); ok &= (gv[k] >> 16) == (p.tag & 0xffffffffffffull); }
        if (__all(ok) || give_up(p, spins)) break;
        __builtin_amdgcn_s_sleep(RETRY_SLEEP); }
      UNR for (int k = 0; k < 4; ++k) *(LAS float*)(smem + L_TAB + (k * 512 + tid) * 4) = bf_lo((unsigned)gv[k]); }
    wait_lgkm0(); bar();
    STAMPL(1, 3);
    const __amdgpu_buffer_rsrc_t rh = mkrs(p.ws + WS_H, 4u << 20);
    UNR for (int i = 0; i < 2; ++i) { const int c0 = i * 512 + lane * 8; LAS char* tb = smem + L_TAB + c0 * 4;
      const f32x4 s0 = *(LAS f32x4*)tb, s1 = *(LAS f32x4*)(tb + 16), c0v = *(LAS f32x4*)(tb + 4096), c1v = *(LAS f32x4*)(tb + 4112);
      const f32x2 sh[4] = {lo2(s0), hi2(s0), lo2(s1), hi2(s1)}, sc[4] = {lo2(c0v), hi2(c0v), lo2(c1v), hi2(c1v)}; u32x4 o;
      UNR for (int j = 0; j < 4; ++j) o[j] = pk2v((v[4 * i + j] * rs) * sc[j] + sh[j]);
      st16_dev(rh, (unsigned)(row * D_ + c0) * 2, o); }
    STAMPL(1, 1);
    wait_vm<0>(); bar();
    STAMPL(1, 4);
    if (tid == 0) post(p, F_H + rb, hp);
    if (wave >= 4) { dma1(sb + L_WPHI + lw * 1024, lane * 16, (const char*)p.wphi + hp * 4096 + lw * 1024); ib(0, 0); }
    if (wave == 0) await(p, F_H + rb, 16, lane);
    bar();
  }
  STAMP(1, 2);
  const int q = r >> 2, p4 = lane & 3; const f32x4 z4 = {0.f, 0.f, 0.f, 0.f};
  const __amdgpu_buffer_rsrc_t rk = mkrs(p.ws + WS_KVP, 2u << 20), rz = mkrs(p.ws + WS_ZP, 131072);
  LAS char* hx = smem + L_HX + lw * 4096 + lane * 16;
  bf16x8 pf[4][2];
  auto load_wf = [&](bf16x8 (&wf)[2]) { UNR for (int ft = 0; ft < 2; ++ft) { const unsigned a = sb + L_WPHI + wn * 2048 + (4 * g + q) * 64 + ft * 32 + p4 * 8; wf[ft] = cat8(ldtr(a), ldtr(a + 1024)); } };
  if (wave >= 4) {
    const char* A = p.ws + WS_H + (size_t)rb * 128 * D_ * 2; const unsigned va = voff_a(lw, lane, D_);
    __builtin_amdgcn_s_setprio(3);
    loader_run<NST, 6, KT, KT - 3, false>([&](int t, int slot) { issue_a<true>(sb + slot * 40960 + lw * 1024, A + ((t + t0) & 15) * 128, D_, va); }, ib);
    wait_vm<10>(); bar(); wait_vm<0>(); bar();
    if (CODEPF && lw == 0) touch_code(sb + L_DUMMY, lane);
    u32x4 w[8];
    UNR for (int i = 0; i < 8; ++i) w[i] = __builtin_nontemporal_load((const u32x4*)(p.w_mod + (size_t)(kb + 32 * i) * 6144 + 2048 + cbb + (tid & 7) * 8));
    bar();
    __builtin_amdgcn_s_setprio(0);
    bf16x8 wf[2]; load_wf(wf);
    bar();
    UNR for (int i = 0; i < 8; ++i) pin(w[i]);
    { const int l = blockIdx.x * 176 + lw * 44 + (lane < 44 ? lane : 0), l2 = l - 16384;
      touch_lds(sb + L_DUMMY, l2 < 0 ? (const char*)p.wo + (size_t)l * 128 : (const char*)p.w_in + (size_t)(l2 / 28) * 7168 + 3584 + (l2 % 28) * 128); }
    fold(w, sl2, 8, smem + L_XZ + 1024 + lw * 256);
    auto qunit = [&](int mt) { f32x4 pp[2][2]; u32x2 o[2][4];
      UNR for (int u = 0; u < 2; ++u) { const bf16x8 qf = *(LAS bf16x8*)(hx + (mt + u) * 1024); pp[u][0] = mfma16(wf[0], qf, z4); pp[u][1] = mfma16(wf[1], qf, z4); }
      softmax_pm2(pp, o);
      UNR for (int u = 0; u < 2; ++u) { pf[mt + u][0] = cat8u(o[u][0], o[u][1]); pf[mt + u][1] = cat8u(o[u][2], o[u][3]); pin(pf[mt + u][0]); pin(pf[mt + u][1]); } };
    bar();
    if (lw == 0) { float t = 0.f; UNR for (int w2 = 0; w2 < 4; ++w2) t += *(LAS float*)(smem + L_XZ + 1024 + (w2 * 64 + lane) * 4);
      st4_dev(mkrs(p.ws + WS_MODB, 65536), (unsigned)((blockIdx.x >> 6) * 4096 + cbb + lane) * 4, __float_as_uint(t)); }
    qunit(0);
    if (lw == 0) wait_vm<0>();
    bar();
    qunit(2);
  } else {
    STAMP(2, 0); STAMPC(0); Cons<3, 1, NST, 4> c; c.init(sb, wm, wn, lane); c.start(); STAMP(2, 1); STAMPC(1); c.run_half(KT); STAMP(2, 2); STAMPC(2);
    bf16x8 wf[2]; load_wf(wf);
    UNR for (int mt = 0; mt < 4; ++mt) *(LAS bf16x8*)(hx + mt * 1024) = pack8(c.acc[mt][0], c.acc[mt][1]);
    wait_lgkm0(); bar();
    STAMP(0, 2);
    LAS char* phi = smem + L_PHI + wave * (64 * 136);
    __builtin_amdgcn_s_setprio(2);
    UNR for (int mt = 0; mt < 4; mt += 2) { f32x4 pp[2][2]; u32x2 o[2][4];
      UNR for (int u = 0; u < 2; ++u) { const bf16x8 kf = pack8(c.acc[mt + u][2], c.acc[mt + u][3]); pp[u][0] = mfma16(wf[0], kf, z4); pp[u][1] = mfma16(wf[1], kf, z4); }
      softmax_pm2(pp, o);
      UNR for (int u = 0; u < 2; ++u) UNR for (int ft = 0; ft < 4; ++ft) *(LAS u32x2*)(phi + (16 * (mt + u) + r) * 136 + (16 * ft + 4 * g) * 2) = o[u][ft]; }
    STAMP(0, 3);
    f32x4 kv[4][2], zz[4];
    { const u32x4 o4 = {0x3f803f80u, 0x3f803f80u, 0x3f803f80u, 0x3f803f80u}; const bf16x8 ones = __builtin_bit_cast(bf16x8, o4);
      UNR for (int kk = 0; kk < 2; ++kk) { const bf16x8 v0 = pack8(c.acc[2 * kk][4], c.acc[2 * kk + 1][4]), v1 = pack8(c.acc[2 * kk][5], c.acc[2 * kk + 1][5]);
        UNR for (int ft = 0; ft < 4; ++ft) { const unsigned a = (unsigned)(size_t)phi + (32 * kk + 4 * g + q) * 136 + ft * 32 + p4 * 8; const bf16x8 pk = cat8(ldtr(a), ldtr(a + 16 * 136));
          kv[ft][0] = mfma16(pk, v0, kk ? kv[ft][0] : z4); kv[ft][1] = mfma16(pk, v1, kk ? kv[ft][1] : z4); zz[ft] = mfma16(pk, ones, kk ? zz[ft] : z4); } } }
    LAS char* xk = smem + L_XKV + lane * 16;
    if (wm) { UNR for (int ft = 0; ft < 4; ++ft) { *(LAS f32x4*)(xk + (wave * 4 + ft) * 1024) = kv[ft][0]; kv[ft][0] = kv[ft][1]; } }
    else { UNR for (int ft = 0; ft < 4; ++ft) *(LAS f32x4*)(xk + (wave * 4 + ft) * 1024) = kv[ft][1]; }
    if (r == 0) { UNR for (int ft = 0; ft < 4; ++ft) *(LAS f32x4*)(smem + L_XZ + wave * 256 + (16 * ft + 4 * g) * 4) = zz[ft]; }
    STAMP(0, 4);
    wait_lgkm0(); bar();
    STAMP(0, 5);
    const int slotid = (2 * hp + wn) * 16 + rb;
    UNR for (int k2 = 0; k2 < 2; ++k2) { const f32x4 a = kv[2 * k2][0] + *(LAS f32x4*)(xk + ((wave ^ 2) * 4 + 2 * k2) * 1024), b = kv[2 * k2 + 1][0] + *(LAS f32x4*)(xk + ((wave ^ 2) * 4 + 2 * k2 + 1) * 1024);
      st16_dev(rk, (unsigned)(slotid * 4 + wm * 2 + k2) * 1024 + lane * 16, __builtin_bit_cast(u32x4, pack8(a, b))); }
    if (wm == 0 && lane < 16) st16_dev(rz, (unsigned)(slotid * 64 + lane * 4) * 4, __builtin_bit_cast(u32x4, *(LAS f32x4*)(smem + L_XZ + wn * 256 + lane * 16) + *(LAS f32x4*)(smem + L_XZ + (2 + wn) * 256 + lane * 16)));
    STAMP(2, 3); STAMPC(3);
    __builtin_amdgcn_s_setprio(0);
    wait_vm<0>(); bar();
    if (tid == 0) post(p, F_KV + hp, rb);
    STAMP(2, 4); STAMPC(4);
    if (wave == 0) await(p, F_KV + hp, 16, lane);
    STAMP(2, 5); STAMPC(5);
  }
  bar();
  STAMP(2, 6); STAMPC(6);
  {
    const int hl = wave >> 2, ck = wave & 3, head = 2 * hp + hl;
    u32x4 v[16]; UNR for (int i = 0; i < 16; ++i) v[i] = ld16_dev(rk, (unsigned)((head * 16 + ((i + t0) & 15)) * 4 + ck) * 1024 + lane * 16);
    if (ck == 0) { float zs = 0.f; unsigned zv[16]; UNR for (int i = 0; i < 16; ++i) zv[i] = ld4_dev(rz, (unsigned)((head * 16 + i) * 64 + lane) * 4);
      UNR for (int i = 0; i < 16; ++i) zs += __uint_as_float(zv[i]);
      *(LAS u16*)(smem + L_ZS + hl * 128 + lane * 2) = (u16)pk2(zs, zs); }
    f32x2 a[4]; UNR for (int j = 0; j < 4; ++j) a[j] = up2(v[0][j]);
    UNR for (int i = 1; i < 16; ++i) UNR for (int j = 0; j < 4; ++j) a[j] += up2(v[i][j]);
    *(LAS u32x4*)(smem + L_KVS + hl * 4096 + ck * 1024 + lane * 16) = (u32x4){pk2v(a[0]), pk2v(a[1]), pk2v(a[2]), pk2v(a[3])};
    wait_lgkm0(); bar();
    STAMP(2, 7); STAMPC(7);
  }
  pre_load(p, tid, pr);
  asm volatile("" ::: "memory");
  if (wave >= 4) { bf16x8 kv[2][2], zf[2];
    UNR for (int dt = 0; dt < 2; ++dt) UNR for (int k2 = 0; k2 < 2; ++k2) kv[dt][k2] = *(LAS bf16x8*)(smem + L_KVS + wn * 4096 + (dt * 2 + k2) * 1024 + lane * 16);
    UNR for (int k2 = 0; k2 < 2; ++k2) { LAS char* a = smem + L_ZS + wn * 128 + (32 * k2 + 4 * g) * 2; zf[k2] = cat8(*(LAS s16x4*)a, *(LAS s16x4*)(a + 32)); }
    UNR for (int mt = 0; mt < 4; ++mt) {
      const f32x4 den = mfma16(zf[1], pf[mt][1], mfma16(zf[0], pf[mt][0], z4)); const float rd = __builtin_amdgcn_rcpf(den[0] + 1e-6f);
      UNR for (int dt = 0; dt < 2; ++dt) { const f32x4 nu = mfma16(kv[dt][1], pf[mt][1], mfma16(kv[dt][0], pf[mt][0], z4)) * rd;
        *(LAS u32x2*)(smem + L_OST + (64 * wm + 16 * mt + r) * 144 + (32 * wn + 16 * dt + 4 * g) * 2) = (u32x2){pk2(nu[0], nu[1]), pk2(nu[2], nu[3])}; }
    }
    wait_lgkm0(); }
  bar();
  { const __amdgpu_buffer_rsrc_t ro = mkrs(p.ws + WS_O, 4u << 20);
    UNR for (int i = 0; i < 2; ++i) { const int row = (tid >> 3) + 64 * i, ch = tid & 7;
      st16_dev(ro, (unsigned)((rb * 128 + row) * D_ + hp * 64 + ch * 8) * 2, *(LAS u32x4*)(smem + L_OST + row * 144 + ch * 16)); } }
  STAMP(2, 8); STAMPC(8);
  publish_and_open(p, wave, tid, F_O + rb, hp, true, [&](int h) { UNR for (int t = 4 * h; t < 4 + 2 * h; ++t) proj_ib<false>(p, sb, lw, lane, t, t); });
  STAMP(2, 9); STAMPC(9);
}

template <bool LAST>
DI void phase_proj(const Prm& p, LAS char* smem, int wave, int lane, int tid, Pre& pr) {
  constexpr int NST = 6, KT = LAST ? 28 : 16, LDA = LAST ? FH : D_;
  const int xcd = blockIdx.x & 7, idx = blockIdx.x >> 3, rb = (xcd >> 1) * 4 + (idx >> 3), cb = (xcd & 1) * 8 + (idx & 7), t0 = (LAST ? PROT4 : PROT2) * (idx >> 3) * (KT / 4);
  const unsigned sb = (unsigned)(size_t)smem; const int r = lane & 15, g = lane >> 4, wm = (wave >> 1) & 1, wn = wave & 1;
  const int ch = tid & 7, col = cb * 64 + ch * 8; u32x4 (&xv)[2] = pr.xv; f32x4 gv[2], cv[2];
  auto tab_read = [&]() { LAS char* t = smem + (LAST ? L_MT4 : L_MT2) + ch * 32; UNR for (int i = 0; i < 2; ++i) { gv[i] = *(LAS f32x4*)(t + 16 * i); if (!LAST) cv[i] = *(LAS f32x4*)(t + 256 + 16 * i); } wait_lgkm0(); };
  if (wave == 0) await(p, (LAST ? F_U : F_O) + rb, LAST ? 14 : 16, lane);
  bar();
  if (wave >= 4) {
    const int lw = wave - 4; const char* A = p.ws + (LAST ? WS_U : WS_O) + (size_t)rb * 128 * LDA * 2; const unsigned va = voff_a(lw, lane, LDA);
    auto rot = [&](int t) { t += t0; return t >= KT ? t - KT : t; };
    auto ib = [&](int t, int slot) { proj_ib<LAST>(p, sb, lw, lane, t, slot); };
    __builtin_amdgcn_s_setprio(3);
    loader_run<NST, 2, KT, KT - 3, true>([&](int t, int slot) { issue_a<true>(sb + slot * 24576 + lw * 1024, A + rot(t) * 128, LDA, va); }, ib);
    __builtin_amdgcn_s_setprio(0);
    wait_vm<6>(); bar(); wait_vm<0>(); bar();
    bar();
    tab_read();
  } else {
    STAMP(2, LAST ? 26 : 10); STAMPC(LAST ? 26 : 10);
    Cons<1, 2, NST, 2> c; c.init(sb, wm, wn, lane); c.start(); STAMP(2, LAST ? 27 : 11); STAMPC(LAST ? 27 : 11); c.run_first2();
    Tab tv; if (wave == 0) tv = tab_load(p, fresh(tid) & 63, LAST ? 3072 : 0, LAST ? 3072 : 2048);
    c.run_pairs(KT - 4);
    if (wave == 0) { tab_store(smem + (LAST ? L_MT4 : L_MT2), fresh(tid) & 63, tv); wait_lgkm0(); SB(); }
    c.run_last2(); STAMP(2, LAST ? 28 : 12); STAMPC(LAST ? 28 : 12);
    bar();
    tab_read();
    UNR for (int mt = 0; mt < 4; ++mt) UNR for (int j = 0; j < 2; ++j)
      *(LAS u32x2*)(smem + (64 * wm + 16 * mt + r) * 144 + (32 * wn + 16 * j + 4 * g) * 2) = (u32x2){pk2(c.acc[mt][j][0], c.acc[mt][j][1]), pk2(c.acc[mt][j][2], c.acc[mt][j][3])};
    wait_lgkm0();
  }
  bar();
  const f32x2 g2[4] = {lo2(gv[0]), hi2(gv[0]), lo2(gv[1]), hi2(gv[1])};
  u32x4 o[2];
  UNR for (int i = 0; i < 2; ++i) {
    const int row = (tid >> 3) + 64 * i; const u32x4 av = *(LAS u32x4*)(smem + row * 144 + ch * 16);
    if (LAST) xv[i] = *(LAS u32x4*)(smem + L_X1 + i * 8192 + tid * 16);
    UNR for (int j = 0; j < 4; ++j) o[i][j] = pk2v(up2(xv[i][j]) + up2(pk2v(g2[j] * up2(av[j]))));
    if (!LAST) *(LAS u32x4*)(smem + L_X1 + i * 8192 + tid * 16) = o[i];
    const size_t off = (size_t)(rb * 128 + row) * D_ + col;
    if (LAST) *(u32x4*)(p.out + off) = o[i];
    else {
      const f32x2 c2[4] = {lo2(cv[0]), hi2(cv[0]), lo2(cv[1]), hi2(cv[1])}; f32x2 v[4]; u32x4 y;
      UNR for (int j = 0; j < 4; ++j) { v[j] = up2(o[i][j]); y[j] = pk2v(v[j] * c2[j]); }
      st16_dev(mkrs(p.ws + WS_Y, 4u << 20), (unsigned)off * 2, y);
      const f32x2 t = (v[0] + v[1]) + (v[2] + v[3]); const float mu = sum8(t[0] + t[1]) * (1.0f / 64.0f); f32x2 q = {0.f, 0.f};
      UNR for (int j = 0; j < 4; ++j) { const f32x2 d = v[j] - mu; q += d * d; }
      const float qs = sum8(q[0] + q[1]);
      if (ch == 0) st8_dev(mkrs(p.ws + WS_ST1, 262144), (unsigned)(((cb >> 1) * 2048 + rb * 128 + row) * 16 + (cb & 1) * 8), (u32x2){__float_as_uint(mu), __float_as_uint(qs)});
    }
  }
  STAMP(2, LAST ? 29 : 13); STAMPC(LAST ? 29 : 13);
  if (!LAST) publish_and_open(p, wave, tid, F_Y + rb, cb, idx < 28, [&](int h) { UNR for (int t = 2 * h; t < 2 + h; ++t) gmlp_ib0(p, sb, wave & 3, lane, t); });
  if (STAMPS && LAST) wait_vm<0>();
  STAMP(2, LAST ? 30 : 14); STAMPC(LAST ? 30 : 14);
}

DI void helper_cs(const Prm& p, LAS char* smem, int j, int tid) {
  LAS float* tab = (LAS float*)(smem + 8192); const int ch = tid & 15, k0 = (j & 1) * 512 + (tid >> 4), wave = tid >> 6;
  const u16* wp = p.w_in + (size_t)k0 * 3584 + (j >> 1) * 128 + ch * 8; u32x4 w[16];
  STAMP(0, 8);
  { const __amdgpu_buffer_rsrc_t r = mkrs(p.ws + WS_MODB, 65536); f32x4 m = {0.f, 0.f, 0.f, 0.f}; u32x4 q[4];
    UNR for (int k = 0; k < 4; ++k) q[k] = ld16_dev(r, (unsigned)(k * 4096 + 1024 + tid * 4) * 4);
    UNR for (int i = 0; i < 16; ++i) w[i] = *(const u32x4*)(wp + FH + (size_t)i * 32 * 3584);
    m = (__builtin_bit_cast(f32x4, q[0]) + __builtin_bit_cast(f32x4, q[1])) + (__builtin_bit_cast(f32x4, q[2]) + __builtin_bit_cast(f32x4, q[3]));
    UNR for (int e = 0; e < 4; ++e) { m[e] = rbf(m[e]); if (tid >= 256) m[e] = rbf(1.0f + m[e]); }
    *(LAS f32x4*)(tab + tid * 4) = m; }
  wait_lgkm0(); bar();
  STAMP(0, 9);
  for (int u = 1; u >= 0; --u) { f32x2 a[4], b[4];
    UNR for (int e = 0; e < 4; ++e) { a[e] = (f32x2){0.f, 0.f}; b[e] = a[e]; }
    UNR for (int i = 0; i < 16; ++i) { const float cc = tab[1024 + k0 + 32 * i], ss = tab[k0 + 32 * i];
      UNR for (int e = 0; e < 4; ++e) { const f32x2 v = up2(w[i][e]); a[e] += v * cc; b[e] += v * ss; } }
    LAS float* red = (LAS float*)smem;
    UNR for (int e = 0; e < 4; ++e) UNR for (int h = 0; h < 2; ++h) { a[e][h] = x16sum(a[e][h]); b[e][h] = x16sum(b[e][h]); }
    if ((tid & 63) < 16) { UNR for (int e = 0; e < 4; ++e) { *(LAS f32x2*)(red + wave * 256 + ch * 8 + 2 * e) = a[e]; *(LAS f32x2*)(red + wave * 256 + 128 + ch * 8 + 2 * e) = b[e]; } }
    wait_lgkm0(); bar();
    if (tid < 64) { f32x4 s = *(LAS f32x4*)(red + tid * 4); UNR for (int w2 = 1; w2 < 8; ++w2) s += *(LAS f32x4*)(red + w2 * 256 + tid * 4);
      st16_dev(mkrs(p.ws + WS_CSB, 65536), (unsigned)((j & 1) * 7168 + (tid >> 5) * 3584 + (14 * u + (j >> 1)) * 128 + (tid & 31) * 4) * 4, __builtin_bit_cast(u32x4, s));
      wait_vm<0>(); if (tid == 0) post(p, F_CS + 14 * u + (j >> 1), j & 1); }
    bar();
    STAMP(0, 11 + 2 * (1 - u));
    if (u) { UNR for (int i = 0; i < 16; ++i) w[i] = *(const u32x4*)(wp + (size_t)i * 32 * 3584); } }
}
DI void helper_touch(const Prm& p, unsigned dummy, int hj, int wave, int tid) {
  const unsigned long long t0 = __builtin_amdgcn_s_memrealtime();
  for (int i = 0; i < 6; ++i) { int l = ((i & 3) * 32 + hj) * 512 + tid; if (i >= 4 && l >= 28672) l -= 16384;
    const char* a = i < 4 ? (const char*)p.w_sp + (size_t)(l & 2047) * 4096 + (l >> 11) * 128 : (const char*)p.w_out + (size_t)l * 128;
    while (__builtin_amdgcn_s_memrealtime() - t0 < (unsigned long long)(PACE * (i * 8 + wave))) __builtin_amdgcn_s_sleep(4);
    touch_lds(dummy, a); wait_vm<1>(); }
  wait_vm<0>();
  STAMP(0, 14);
}
DI void phase_gmlp(const Prm& p, LAS char* smem, int wave, int lane, int tid) {
  constexpr int NST = 3, L_IMG = 98304, L_TB1 = 131072, L_TB2 = 132096, L_TCS = 133120, L_ST = 135168, L_G = 32768;
  const int xcd = blockIdx.x & 7, idx = blockIdx.x >> 3;
  if (idx >= 28) { const int j = (idx - 28) * 8 + xcd; if (j < 28) helper_cs(p, smem, j, tid);
    helper_touch(p, (unsigned)(size_t)smem + 65536, j, wave, tid);
    publish_and_open(p, wave, tid, -1, 0, true, [&](int h) { UNR for (int t = 4 * h; t < 4 + 2 * h; ++t) proj_ib<true>(p, (unsigned)(size_t)smem, wave & 3, lane, t, t); });
    return; }
  const int rb = (xcd >> 1) * 4 + idx / 7, cb = (xcd & 1) * 7 + idx % 7, t0 = ZROT * 4 * (idx / 7);
  auto chunk_at = [&](int base, int row, int ch) { return (LAS u32x4*)(smem + base + row * 256 + ((ch ^ (row & 15)) * 16)); };
  auto normalize = [&]() {
    const int tid = fresh(threadIdx.x); const __amdgpu_buffer_rsrc_t ro = mkrs(p.ws + WS_Z2N, 8u << 20);
    UNR for (int h = 0; h < 2; ++h) { u32x4 v[4]; f32x2 st[4];
      UNR for (int i = 0; i < 4; ++i) { const int row = 64 * h + 16 * i + (tid >> 4); v[i] = *chunk_at(L_IMG, row, tid & 15); st[i] = *(LAS f32x2*)(smem + L_TB2 + row * 8); }
      UNR for (int i = 0; i < 4; ++i) { UNR for (int e = 0; e < 4; ++e) v[i][e] = pk2v(up2(v[i][e]) * st[i][0] + st[i][1]);
        st16_dev(ro, (unsigned)((rb * 128 + 64 * h + 16 * i + (tid >> 4)) * FH + cb * 128 + (tid & 15) * 8) * 2, v[i]); } } };
  float bs[4];
  if (wave >= 6) {
    const int row = tid - 384; const unsigned so = (unsigned)(rb * 128 + row) * 16;
    auto bars = [&](int n) { for (int i = 0; i < n; ++i) bar(); };
    bar();
    { u32x4 sv[8]; const __amdgpu_buffer_rsrc_t rs = mkrs(p.ws + WS_ST1, 262144);
      wait_vm<0>(); bar(); UNR for (int i = 0; i < 8; ++i) sv[i] = ld16_dev(rs, so + i * 32768);
      bars(3); *(LAS f32x2*)(smem + L_TB1 + row * 8) = stats_comb<8, 64>(sv); wait_lgkm0(); }
    auto cs_load = [&](int u, u32x4& c0, u32x4& c1) { const __amdgpu_buffer_rsrc_t rc = mkrs(p.ws + WS_CSB, 65536); const unsigned o = (unsigned)(u * FH + cb * 128 + ((row >> 5) & 1) * 3584 + (row & 31) * 4) * 4; c0 = ld16_dev(rc, o); c1 = ld16_dev(rc, o + 7168 * 4); };
    if (CODEPF && wave == 6) touch_code((unsigned)(size_t)smem + 137216, lane);
    bars(9);
    { const unsigned long long s1 = peek(p, F_CS + 14 + cb, 2, lane); bar(); if (!__all(s1 == p.tag)) await(p, F_CS + 14 + cb, 2, lane); asm volatile("" ::: "memory");
      u32x4 c0, c1; cs_load(1, c0, c1); bars(2);
      *(LAS f32x4*)(smem + L_TCS + (row & 63) * 16) = __builtin_bit_cast(f32x4, c0) + __builtin_bit_cast(f32x4, c1); wait_lgkm0(); }
    bars(2);
    { const f32x4 s4 = *(LAS f32x4*)(smem + L_ST + row * 16); const float d = s4[0] - s4[2];
      st8_dev(mkrs(p.ws + WS_ST2, 7 * 2048 * 16), (cb >> 1) * 32768 + so + (cb & 1) * 8, (u32x2){__float_as_uint(0.5f * (s4[0] + s4[2])), __float_as_uint(s4[1] + s4[3] + 32.0f * d * d)}); }
    bar(); wait_vm<0>(); bar();
    if (row == 0) post(p, F_S2 + rb, cb);
    STAMPS2(12); bar();
    { const unsigned long long s1 = peek(p, F_S2 + rb, 14, lane); bar(); STAMPS2(14); if (!__all(s1 == p.tag)) await(p, F_S2 + rb, 14, lane); asm volatile("" ::: "memory"); STAMPS2(15);
      u32x4 sv[7]; const __amdgpu_buffer_rsrc_t rs = mkrs(p.ws + WS_ST2, 7 * 2048 * 16); UNR for (int i = 0; i < 7; ++i) sv[i] = ld16_dev(rs, so + i * 32768);
      bars(2); const f32x2 st = stats_comb<7, 128>(sv); *(LAS f32x2*)(smem + L_TB2 + row * 8) = (f32x2){st[1], -st[0] * st[1]}; wait_lgkm0(); }
    bar(); STAMPS2(16); bar(); STAMPS2(17);
    { const unsigned long long s2 = peek(p, F_CS + cb, 2, lane); bars(2); if (!__all(s2 == p.tag)) await(p, F_CS + cb, 2, lane); asm volatile("" ::: "memory");
      u32x4 c0, c1; cs_load(0, c0, c1); bar();
      if (row == 0) post(p, F_Z + cb, rb);
      STAMPS2(18); bar();
      *(LAS f32x4*)(smem + L_TCS + 1024 + (row & 63) * 16) = __builtin_bit_cast(f32x4, c0) + __builtin_bit_cast(f32x4, c1); wait_lgkm0(); }
    bar(); STAMPS2(19); await(p, F_Z + cb, 16, lane); STAMPS2(20);
    bars(26);
    if (CODEPF && wave == 6) touch_code((unsigned)(size_t)smem + 137216, lane);
    bars(8);
    UNR for (int i = 0; i < 4; ++i) bs[i] = bf_lo(p.b_sp[rb * 128 + 32 * i + (tid >> 4)]);
  } else if (wave >= 4) {
    const int lw = wave - 4; const char* A1 = p.ws + WS_Y + (size_t)rb * 128 * D_ * 2; const char* A2 = (const char*)p.w_sp + (size_t)rb * 128 * S_ * 2;
    const char* B1 = (const char*)p.w_in + cb * 256; const char* B2 = p.ws + WS_Z2N + cb * 256;
    const unsigned va1 = voff_a(lw, lane, D_), va2 = voff_a(lw, lane, S_), vb1 = voff_b(lw, lane, 3584), vb2 = voff_b(lw, lane, FH);
    const unsigned s0 = (unsigned)(size_t)smem + lw * 1024;
    auto ia = [&](int t, int slot) { const unsigned s = s0 + slot * 32768;
      if (t < 32) { const char* a = A1 + ((t + t0) & 15) * 128; dma4h<true>(s, va1, a, 32 * D_); dma4h<true>(s + 8192, va1, a + 128 * D_, 32 * D_); }
      else { const char* a = A2 + (t - 32) * 128; dma4h<false>(s, va2, a, 32 * S_); dma4h<false>(s + 8192, va2, a + 128 * S_, 32 * S_); } };
    auto ib = [&](int t, int slot) { const unsigned s = s0 + slot * 32768 + 16384;
      if (t < 32) { const char* b = B1 + (size_t)((t + t0) & 15) * 64 * 3584 * 2 + (t < 16 ? FH * 2 : 0); dma4h<false>(s, vb1, b, 32 * 3584); dma4h<false>(s + 8192, vb1, b + 128, 32 * 3584); }
      else { const char* b = B2 + (size_t)(t - 32) * 64 * FH * 2; dma4h<true>(s, vb2, b, 32 * FH); dma4h<true>(s + 8192, vb2, b + 128, 32 * FH); } };
    int slot = 0;
    auto steps = [&](int b0, int b1) { for (int b = b0; b <= b1; ++b) { wait_vm<16>(); bar(); ia(b + 2, slot); ib(b + 2, slot); slot = (slot == 2) ? 0 : slot + 1; } };
    bar();
    __builtin_amdgcn_s_setprio(3);
    ia(0, 0); ia(1, 1); wait_vm<8>(); bar();
    ia(2, 2); wait_vm<8>(); bar(); ia(3, 0); ib(3, 0); slot = 1;
    STAMPL(1, 8); steps(2, 12); STAMPL(1, 9); steps(13, 16); STAMPL(1, 10); steps(17, 24);
    bar();
    steps(25, 33); STAMPL(1, 21); steps(34, 61); STAMPL(1, 23);
    wait_vm<16>(); bar(); wait_vm<0>(); bar();
    __builtin_amdgcn_s_setprio(0);
    UNR for (int i = 0; i < 4; ++i) bs[i] = bf_lo(p.b_sp[rb * 128 + 32 * i + (tid >> 4)]);
  } else {
    const unsigned sb = (unsigned)(size_t)smem; const int wm = wave >> 1, wn = wave & 1;
    Cons<2, 2, NST, 4> c; c.init(sb, wm, wn, lane);
    auto frag_at = [&](int base, int mt, int j, int r, int g) { const int row = 64 * wm + 16 * mt + r, ch = 8 * (j >> 1) + 4 * wn + 2 * (j & 1) + (g >> 1); return (LAS u32x2*)(smem + base + row * 256 + ((ch ^ (row & 15)) * 16) + (g & 1) * 8); };
    auto leave = [&](bool z2) {
      mfma_drain();
      f32x4 cs[4], bi[4]; f32x2 st[4]; const int l2 = fresh(threadIdx.x), r = l2 & 15, g = (l2 >> 4) & 3;
      UNR for (int j = 0; j < 4; ++j) { LAS char* t = smem + L_TCS + (z2 ? 0 : 1024) + (64 * (j >> 1) + 32 * wn + 16 * (j & 1) + 4 * g) * 4; cs[j] = *(LAS f32x4*)t; bi[j] = *(LAS f32x4*)(t + 512); }
      UNR for (int mt = 0; mt < 4; ++mt) st[mt] = *(LAS f32x2*)(smem + L_TB1 + (64 * wm + 16 * mt + r) * 8);
      UNR for (int mt = 0; mt < 4; ++mt) { f32x2 z[8], w[8];
        UNR for (int j = 0; j < 4; ++j) { z[2 * j] = lo2(c.acc[mt][j]) - lo2(cs[j]) * st[mt][0]; z[2 * j + 1] = hi2(c.acc[mt][j]) - hi2(cs[j]) * st[mt][0]; }
        SB();
        UNR for (int j = 0; j < 4; ++j) { z[2 * j] = z[2 * j] * st[mt][1] + lo2(bi[j]); z[2 * j + 1] = z[2 * j + 1] * st[mt][1] + hi2(bi[j]); }
        SB();
        { const float k1 = -2.0f * 0.7978845608028654f * 1.4426950408889634f, k3 = k1 * 0.044715f;
          UNR for (int e = 0; e < 8; ++e) w[e] = z[e] * z[e];
          SB(); UNR for (int e = 0; e < 8; ++e) w[e] = w[e] * k3 + k1;
          SB(); UNR for (int e = 0; e < 8; ++e) w[e] *= z[e];
          SB(); UNR for (int e = 0; e < 8; ++e) w[e] = ex2(w[e]);
          SB(); UNR for (int e = 0; e < 8; ++e) w[e] += 1.0f;
          SB(); UNR for (int e = 0; e < 8; ++e) w[e] = (f32x2){__builtin_amdgcn_rcpf(w[e][0]), __builtin_amdgcn_rcpf(w[e][1])};
          SB(); UNR for (int e = 0; e < 8; ++e) { z[e] *= w[e]; pin(z[e]); }
          SB(); }
        if (z2) { const f32x2 t = ((z[0] + z[1]) + (z[2] + z[3])) + ((z[4] + z[5]) + (z[6] + z[7])); float m = t[0] + t[1];
          UNR for (int j = 0; j < 4; ++j) *frag_at(L_IMG, mt, j, r, g) = (u32x2){pk2v(z[2 * j]), pk2v(z[2 * j + 1])};
          m = x16sum(m) * (1.0f / 64.0f); f32x2 q0 = {0.f, 0.f}, q1 = q0;
          UNR for (int e = 0; e < 4; ++e) { const f32x2 d0 = z[2 * e] - m, d1 = z[2 * e + 1] - m; q0 += d0 * d0; q1 += d1 * d1; }
          q0 += q1; const float qs = x16sum(q0[0] + q0[1]);
          if (g == 0) *(LAS f32x2*)(smem + L_ST + (64 * wm + 16 * mt + r) * 16 + wn * 8) = (f32x2){m, qs}; }
        else { UNR for (int j = 0; j < 4; ++j) *frag_at(L_IMG, mt, j, r, g) = (u32x2){pk2v(z[2 * j]), pk2v(z[2 * j + 1])}; } }
      wait_lgkm0(); SB(); };
    if (wave == 0) await(p, F_Y + rb, 16, lane);
    bar();
    STAMP(2, 15); STAMPC(15); c.start(); STAMP(2, 16); STAMPC(16); c.run_first2(); c.run_pairs(14); STAMP(2, 17); STAMPC(17);
    leave(true);
    STAMP(2, 19); STAMPC(19); c.run_first2(); c.run_pairs(6);
    bar(); normalize(); SB();
    c.run_pairs(2); wait_vm<0>(); c.run_pairs(6); STAMP(2, 22); STAMPC(22);
    leave(false);
    STAMP(2, 23); STAMPC(23);
    c.run_first2(); c.run_pairs(28);
    { const int t2 = fresh(threadIdx.x); UNR for (int i = 0; i < 4; ++i) bs[i] = bf_lo(p.b_sp[rb * 128 + 32 * i + (t2 >> 4)]); }
    c.run_last2(); STAMP(2, 24); STAMPC(24);
    const int l2 = fresh(threadIdx.x);
    UNR for (int mt = 0; mt < 4; ++mt) UNR for (int j = 0; j < 4; ++j) *frag_at(L_G, mt, j, l2 & 15, (l2 >> 4) & 3) = (u32x2){pk2(c.acc[mt][j][0], c.acc[mt][j][1]), pk2(c.acc[mt][j][2], c.acc[mt][j][3])};
    wait_lgkm0();
  }
  bar();
  { const __amdgpu_buffer_rsrc_t ro = mkrs(p.ws + WS_U, 8u << 20); const int tid = fresh(threadIdx.x);
    UNR for (int i = 0; i < 4; ++i) { const int row = 32 * i + (tid >> 4), ch = tid & 15; const u32x4 z = *chunk_at(L_IMG, row, ch), gg = *chunk_at(L_G, row, ch); u32x4 o;
      UNR for (int e = 0; e < 4; ++e) o[e] = pk2v(up2(z[e]) * (up2(gg[e]) + bs[i]));
      st16_dev(ro, (unsigned)((rb * 128 + row) * FH + cb * 128 + ch * 8) * 2, o); } }
  STAMP(2, 21); STAMPC(21);
  publish_and_open(p, wave, tid, F_U + rb, cb, true, [&](int h) { UNR for (int t = 4 * h; t < 4 + 2 * h; ++t) proj_ib<true>(p, (unsigned)(size_t)smem, wave & 3, lane, t, t); });
  STAMP(2, 25); STAMPC(25);
}

__global__ __launch_bounds__(512, 2) void k_main(Args args) {
  extern __shared__ __attribute__((aligned(16))) char smem_[];
  LAS char* smem = (LAS char*)smem_;
  const int tid = threadIdx.x, lane = tid & 63, wave = __builtin_amdgcn_readfirstlane(tid >> 6);
  Prm p; (Args&)p = args;
  asm volatile(".Lkm_begin:");
  STAMP(0, 0);
  if (wave == 2) touch_code((unsigned)(size_t)smem + L_DUMMY, lane);
  Pre pr;
  phase_qkv(p, smem, wave, lane, tid, pr);
  phase_proj<false>(p, smem, wave, fresh(tid) & 63, fresh(tid), pr);
  phase_gmlp(p, smem, wave, fresh(tid) & 63, fresh(tid));
  phase_proj<true>(p, smem, wave, fresh(tid) & 63, fresh(tid), pr);
  if (blockIdx.x == 0 && threadIdx.x == 0) __hip_atomic_store(flag_at(p, F_EPOCH), p.tag, RLX_AGENT);
  asm volatile(".Lkm_end:");
}

extern "C" void kernel_launch(void* const* d_in, const int* in_sizes, int n_in, void* d_out, int out_size, void* d_ws, size_t ws_size, hipStream_t stream) {
  Args p;
  p.x = (const u16*)d_in[0]; p.wqkv = (const u16*)d_in[1]; p.wphi = (const u16*)d_in[2]; p.wo = (const u16*)d_in[3];
  p.w_in = (const u16*)d_in[5]; p.w_sp = (const u16*)d_in[6]; p.b_sp = (const u16*)d_in[7]; p.w_out = (const u16*)d_in[8];
  p.c = (const u16*)d_in[9]; p.w_mod = (const u16*)d_in[10]; p.out = (u16*)d_out; p.ws = (char*)d_ws;
  (void)hipFuncSetAttribute((const void*)k_main, hipFuncAttributeMaxDynamicSharedMemorySize, 163840);
  k_main<<<256, 512, 163840, stream>>>(p);
}
```

```cpp
#include <hip/hip_runtime.h>
#include <stdint.h>

typedef unsigned short u16;
typedef __bf16 bf16x8 __attribute__((ext_vector_type(8)));
typedef __bf16 bf16x2 __attribute__((ext_vector_type(2)));
typedef short s16x4 __attribute__((ext_vector_type(4)));
typedef float f32x2 __attribute__((ext_vector_type(2)));
typedef float f32x4 __attribute__((ext_vector_type(4)));
typedef unsigned u32x4 __attribute__((ext_vector_type(4)));
typedef unsigned u32x2 __attribute__((ext_vector_type(2)));
#define LAS __attribute__((address_space(3)))
#define GAS __attribute__((address_space(1)))
#define DI __device__ __forceinline__
#define SB() __builtin_amdgcn_sched_barrier(0)
#define RLX_AGENT __ATOMIC_RELAXED, __HIP_MEMORY_SCOPE_AGENT
#define UNR _Pragma("unroll")

constexpr int S_ = 2048, D_ = 1024, FH = 1792;
constexpr size_t WS_FLG  = 1u << 20;
constexpr size_t WS_MODA = 0;
constexpr size_t WS_MODB = WS_MODA + 131072;
constexpr size_t WS_CSB  = WS_MODB + 65536;
constexpr size_t WS_ZP   = WS_CSB + 65536;
constexpr size_t WS_ST1  = WS_ZP + 131072;
constexpr size_t WS_ST2  = WS_ST1 + 131072;
constexpr size_t WS_ST0  = WS_FLG + 262144;
constexpr size_t WS_SLOW = (2u << 20) - 524288;
constexpr size_t WS_KVP  = 2u << 20;
constexpr size_t WS_H    = WS_KVP + (2u << 20);
constexpr size_t WS_O    = WS_H + (4u << 20);
constexpr size_t WS_Y    = WS_O + (4u << 20);
constexpr size_t WS_Z2N  = WS_Y + (4u << 20);
constexpr size_t WS_U    = WS_Z2N + (8u << 20);
static_assert(WS_ST2 + 14 * 2048 * 16 <= WS_FLG && WS_ST0 + 262144 <= WS_SLOW, "ws map");
constexpr int F_KV = 0, F_O = 16, F_Y = 32, F_Z = 48, F_U = 64, F_H = 80, F_MB = 96, F_CS = 112, F_EPOCH = 126, F_TMO = 127;
constexpr unsigned SPIN_CAP = 1u << 20;

#define HINT_SLEEP 12
#define RETRY_SLEEP 8
#define S2_SLEEP 12
#define PROG 15
#define GB1 5
#define GB2 10
#define XB 15
#define BSC 1
#define FSP 16
#define POLL_SLEEP 1
#define S0SP 128
#define PACE 20
#define CODEPF 1
struct Args { const u16 *x, *wqkv, *wphi, *wo, *w_in, *w_sp, *b_sp, *w_out, *c, *w_mod; u16* out; char* ws; };
struct Prm : Args { unsigned long long tag; };

template <int N> DI void wait_vm() { asm volatile("s_waitcnt vmcnt(%0)" ::"n"(N) : "memory"); }
DI void wait_lgkm0() { __builtin_amdgcn_s_waitcnt(0xC07F); }
DI void bar() { asm volatile("" ::: "memory"); __builtin_amdgcn_s_barrier(); asm volatile("" ::: "memory"); }
#define DMA_OP(sfx, n, off) "s_add_i32 m0, %1, " off "\n\ts_nop 0\n\tglobal_load_lds_dwordx4 %2, %" n sfx "\n\t"
#define DMA4_STR(sfx) "s_nop 4\n\ts_mov_b32 %0, m0\n\t" DMA_OP(sfx, "3", "0") DMA_OP(sfx, "4", "0x1000") DMA_OP(sfx, "5", "0x2000") DMA_OP(sfx, "6", "0x3000") "s_mov_b32 m0, %0"
#define DMA2_STR(sfx) "s_nop 4\n\ts_mov_b32 %0, m0\n\t" DMA_OP(sfx, "3", "0") DMA_OP(sfx, "4", "0x1000") "s_mov_b32 m0, %0"
template <bool DEV> DI void dma4(unsigned lds, unsigned voff, const char* b0, const char* b1, const char* b2, const char* b3) { unsigned keep;
  if (DEV) asm volatile(DMA4_STR(" sc1") : "=&s"(keep) : "s"(lds), "v"(voff), "s"(b0), "s"(b1), "s"(b2), "s"(b3) : "memory", "scc");
  else asm volatile(DMA4_STR("") : "=&s"(keep) : "s"(lds), "v"(voff), "s"(b0), "s"(b1), "s"(b2), "s"(b3) : "memory", "scc"); }
#define DMA4H_STR(sfx) "s_nop 4\n\ts_mov_b32 %0, m0\n\t" DMA_OP(sfx, "3", "0") DMA_OP(sfx, "4", "0x800") DMA_OP(sfx, "5", "0x1000") DMA_OP(sfx, "6", "0x1800") "s_mov_b32 m0, %0"
template <bool DEV> DI void dma4h(unsigned lds, unsigned voff, const char* b, size_t d) { unsigned keep;
  if (DEV) asm volatile(DMA4H_STR(" sc1") : "=&s"(keep) : "s"(lds), "v"(voff), "s"(b), "s"(b + d), "s"(b + 2 * d), "s"(b + 3 * d) : "memory", "scc");
  else asm volatile(DMA4H_STR("") : "=&s"(keep) : "s"(lds), "v"(voff), "s"(b), "s"(b + d), "s"(b + 2 * d), "s"(b + 3 * d) : "memory", "scc"); }
template <bool DEV> DI void dma2(unsigned lds, unsigned voff, const char* b0, const char* b1) { unsigned keep;
  if (DEV) asm volatile(DMA2_STR(" sc1") : "=&s"(keep) : "s"(lds), "v"(voff), "s"(b0), "s"(b1) : "memory", "scc");
  else asm volatile(DMA2_STR("") : "=&s"(keep) : "s"(lds), "v"(voff), "s"(b0), "s"(b1) : "memory", "scc"); }
DI void dma1(unsigned lds, unsigned voff, const char* b0) { unsigned keep;
  asm volatile("s_nop 4\n\ts_mov_b32 %0, m0\n\ts_mov_b32 m0, %1\n\ts_nop 0\n\tglobal_load_lds_dwordx4 %2, %3\n\ts_mov_b32 m0, %0" : "=&s"(keep) : "s"(lds), "v"(voff), "s"(b0) : "memory"); }
DI void touch_lds(unsigned lds, const char* p) { unsigned keep;
  asm volatile("s_mov_b32 %0, m0\n\ts_mov_b32 m0, %1\n\ts_nop 0\n\tglobal_load_lds_dword %2, off\n\ts_mov_b32 m0, %0" : "=&s"(keep) : "s"(lds), "v"(p) : "memory"); }
DI void touch_code(unsigned lds, int lane) { unsigned long long pc; unsigned back, len;
  asm volatile("s_getpc_b64 %0\n1:\n\ts_mov_b32 %1, 1b-.Lkm_begin\n\ts_mov_b32 %2, .Lkm_end-.Lkm_begin" : "=s"(pc), "=s"(back), "=s"(len));
  const unsigned l = ((blockIdx.x >> 3) + 32 * (lane & 31)) * 128; touch_lds(lds, (const char*)(pc - back) + (l < len ? l : 0)); }
DI int slow_col(const void* base) { return (3 - (int)((size_t)base >> 7)) & 7; }
constexpr int L_DUMMY = 143360, L_X1 = 147456;
DI unsigned pk2(float a, float b) { f32x2 v = {a, b}; return __builtin_bit_cast(unsigned, __builtin_convertvector(v, bf16x2)); }
DI float bf_lo(unsigned u) { return __uint_as_float(u << 16); }
DI float bf_hi(unsigned u) { return __uint_as_float(u & 0xffff0000u); }
DI float rbf(float a) { return bf_lo(pk2(a, a)); }
DI bf16x8 pack8(const f32x4& a, const f32x4& b) { u32x4 r = {pk2(a[0], a[1]), pk2(a[2], a[3]), pk2(b[0], b[1]), pk2(b[2], b[3])}; return __builtin_bit_cast(bf16x8, r); }
DI f32x4 mfma16(bf16x8 a, bf16x8 b, f32x4 c) { return __builtin_amdgcn_mfma_f32_16x16x32_bf16(a, b, c, 0, 0, 0); }
DI void mfma_acc(f32x4& c, bf16x8 a, bf16x8 b, bool first) {
  if (first) asm volatile("v_mfma_f32_16x16x32_bf16 %0, %1, %2, 0" : "=&v"(c) : "v"(a), "v"(b));
  else asm volatile("v_mfma_f32_16x16x32_bf16 %0, %1, %2, %0" : "+v"(c) : "v"(a), "v"(b)); }
DI void mfma_drain() { SB(); asm volatile("s_nop 7\n\ts_nop 7"); SB(); }
DI s16x4 ldtr(unsigned a) { return __builtin_amdgcn_ds_read_tr16_b64_v4i16((LAS s16x4*)(size_t)a); }
DI bf16x8 cat8(s16x4 lo, s16x4 hi) { return __builtin_bit_cast(bf16x8, __builtin_shufflevector(lo, hi, 0, 1, 2, 3, 4, 5, 6, 7)); }
template <int CTRL> DI float dpp(float x) { return __builtin_bit_cast(float, __builtin_amdgcn_mov_dpp(__builtin_bit_cast(int, x), CTRL, 0xf, 0xf, true)); }
DI float x16max(float x) { auto s = __builtin_amdgcn_permlane16_swap(__float_as_uint(x), __float_as_uint(x), false, false); x = fmaxf(__uint_as_float(s[0]), __uint_as_float(s[1]));
  auto t = __builtin_amdgcn_permlane32_swap(__float_as_uint(x), __float_as_uint(x), false, false); return fmaxf(__uint_as_float(t[0]), __uint_as_float(t[1])); }
DI float x16min(float x) { auto s = __builtin_amdgcn_permlane16_swap(__float_as_uint(x), __float_as_uint(x), false, false); x = fminf(__uint_as_float(s[0]), __uint_as_float(s[1]));
  auto t = __builtin_amdgcn_permlane32_swap(__float_as_uint(x), __float_as_uint(x), false, false); return fminf(__uint_as_float(t[0]), __uint_as_float(t[1])); }
DI float x16sum(float x) { auto s = __builtin_amdgcn_permlane16_swap(__float_as_uint(x), __float_as_uint(x), false, false); x = __uint_as_float(s[0]) + __uint_as_float(s[1]);
  auto t = __builtin_amdgcn_permlane32_swap(__float_as_uint(x), __float_as_uint(x), false, false); return __uint_as_float(t[0]) + __uint_as_float(t[1]); }
DI float sum8(float x) { x += dpp<0xB1>(x); x += dpp<0x4E>(x); x += dpp<0x141>(x); return x; }
DI float sum16(float x) { x = sum8(x); x += dpp<0x128>(x); return x; }
DI float sum64(float x) { return x16sum(sum16(x)); }
DI __amdgpu_buffer_rsrc_t mkrs(const void* p, unsigned bytes) { return __builtin_amdgcn_make_buffer_rsrc((void*)p, 0, bytes, 0x00020000); }
DI void st16_dev(__amdgpu_buffer_rsrc_t r, unsigned off, u32x4 v) { __builtin_amdgcn_raw_buffer_store_b128(v, r, off, 0, 16); }
DI void st4_dev(__amdgpu_buffer_rsrc_t r, unsigned off, unsigned v) { __builtin_amdgcn_raw_buffer_store_b32(v, r, off, 0, 16); }
DI void st8_dev(__amdgpu_buffer_rsrc_t r, unsigned off, u32x2 v) { __builtin_amdgcn_raw_buffer_store_b64(v, r, off, 0, 16); }
DI u32x4 ld16_dev(__amdgpu_buffer_rsrc_t r, unsigned off) { return __builtin_amdgcn_raw_buffer_load_b128(r, off, 0, 16); }
DI unsigned ld4_dev(__amdgpu_buffer_rsrc_t r, unsigned off) { return __builtin_amdgcn_raw_buffer_load_b32(r, off, 0, 16); }
typedef GAS unsigned long long gu64;
DI gu64* flag_at(const Prm& p, int line, int slot = 0) { return (gu64*)(p.ws + WS_FLG + line * 2048) + slot * FSP; }
DI void post(const Prm& p, int line, int slot) { __hip_atomic_store(flag_at(p, line, slot), p.tag, RLX_AGENT); }
DI unsigned long long peek(const Prm& p, int line, int n, int lane) { return __hip_atomic_load(flag_at(p, line, lane < n ? lane : 0), RLX_AGENT); }
DI u32x4 tag2(const Prm& p, float a, float b) { const unsigned tl = (unsigned)p.tag << 24, th = (unsigned)(p.tag >> 8);
  return (u32x4){tl | ((__float_as_uint(a) + 0x80u) >> 8), th, tl | ((__float_as_uint(b) + 0x80u) >> 8), th}; }
DI bool tag2_ok(const Prm& p, const u32x4& v) { const unsigned tl = (unsigned)p.tag << 24, th = (unsigned)(p.tag >> 8); return ((v[0] ^ tl) | (v[2] ^ tl)) < 0x1000000u && v[1] == th && v[3] == th; }
DI f32x2 tag2_val(const u32x4& v) { return (f32x2){__uint_as_float(v[0] << 8), __uint_as_float(v[2] << 8)}; }
DI bool give_up(const Prm& p, unsigned& spins) {
  __builtin_amdgcn_s_sleep(POLL_SLEEP);
  if (++spins > SPIN_CAP) { __hip_atomic_store(flag_at(p, F_TMO), p.tag, RLX_AGENT); return true; }
  return (spins & 255u) == 0u && __all(__hip_atomic_load(flag_at(p, F_TMO), RLX_AGENT) == p.tag);
}
DI void await_set(const Prm& p, int line, unsigned set, int lane) {
  const int l = lane & 15; const bool skip = !((set >> l) & 1u);
  for (unsigned spins = 0;;) { if (__all(skip || __hip_atomic_load(flag_at(p, line, l), RLX_AGENT) == p.tag) || give_up(p, spins)) break; }
  asm volatile("" ::: "memory");
}
DI void await(const Prm& p, int line, int n, int lane) {
  for (unsigned spins = 0;;) { if (__all(peek(p, line, n, lane) == p.tag) || give_up(p, spins)) break; }
  asm volatile("" ::: "memory");
}
DI float ln_sd(float var) { return rbf(__builtin_amdgcn_sqrtf(rbf(rbf(var) + 1e-5f))); }
DI float ln_rs(float var) { return __builtin_amdgcn_rcpf(ln_sd(var)); }
DI float ln_mu(float m) { return rbf(m); }
DI void swap16(float& a, float& b) { auto s = __builtin_amdgcn_permlane16_swap(__float_as_uint(a), __float_as_uint(b), false, false); a = __uint_as_float(s[0]); b = __uint_as_float(s[1]); }
DI void swap32(float& a, float& b) { auto s = __builtin_amdgcn_permlane32_swap(__float_as_uint(a), __float_as_uint(b), false, false); a = __uint_as_float(s[0]); b = __uint_as_float(s[1]); }
DI f32x2 lo2(const f32x4& v) { return __builtin_shufflevector(v, v, 0, 1); }
DI f32x2 hi2(const f32x4& v) { return __builtin_shufflevector(v, v, 2, 3); }
DI f32x2 ex2(f32x2 v) { return (f32x2){__builtin_amdgcn_exp2f(v[0]), __builtin_amdgcn_exp2f(v[1])}; }
DI f32x2 up2(unsigned u) { return (f32x2){bf_lo(u), bf_hi(u)}; }
DI unsigned pk2v(f32x2 v) { return __builtin_bit_cast(unsigned, __builtin_convertvector(v, bf16x2)); }
DI f32x2 gelu2(f32x2 x) {
  const float k1 = -2.0f * 0.7978845608028654f * 1.4426950408889634f, k3 = k1 * 0.044715f;
  const f32x2 d = ex2(x * ((x * x) * k3 + k1)) + 1.0f;
  return x * (f32x2){__builtin_amdgcn_rcpf(d[0]), __builtin_amdgcn_rcpf(d[1])};
}
DI int fresh(int v) { asm volatile("" : "+v"(v)); return v; }
template <class T> DI void pinv(T& v) { asm volatile("" : "+v"(v)); }
template <class T> DI void pin(T& v) { asm volatile("" : "+v"(v) :: "memory"); }
struct LdAddr { unsigned a, b; };
DI unsigned voff_a(int lw, int lane, int lda) { const int r = 8 * lw + (lane >> 3); return (unsigned)(r * lda * 2 + (((lane & 7) ^ ((r >> 1) & 7)) * 16)); }
DI unsigned voff_b(int lw, int lane, int ldb) { const int rip = lane >> 3, f = ((rip >> 1) & 1) | ((lw & 1) << 1); return (unsigned)((8 * lw + rip) * ldb * 2 + (((lane & 7) ^ (f << 1)) * 16)); }
template <bool DEV> DI void issue_a(unsigned s, const char* ga, int lda, unsigned va) { const size_t d = (size_t)64 * lda; dma4<DEV>(s, va, ga, ga + d, ga + 2 * d, ga + 3 * d); }
template <int NP, bool DEV> DI void issue_b(unsigned s, const char* gb, int ldb, int pstride, unsigned vb) { const size_t d = (size_t)64 * ldb, e = (size_t)2 * pstride;
  if (NP == 1) dma2<DEV>(s + 16384, vb, gb, gb + d);
  else dma4<DEV>(s + 16384, vb, gb, gb + d, gb + e, gb + e + d);
  if (NP == 3) dma2<DEV>(s + 32768, vb, gb + 2 * e, gb + 2 * e + d); }
template <int NST, int PA, int PB, int KT, int LAST, class FA, class FB>
DI void loader_run(FA ia, FB ib) {
  constexpr int P = PA + PB;
  ia(0, 0); ia(1, 1);
  wait_vm<PA>(); bar();
  UNR for (int t = 2; t < NST; ++t) ia(t, t);
  UNR for (int b = 1; b < NST; ++b) {
    if (b == 1) { wait_vm<(NST - 2) * PA>(); }
    else if (b == 2) { wait_vm<(NST > 2 ? (NST - 3) * PA + P : 0)>(); }
    else if (b == 3) { wait_vm<(NST > 3 ? (NST - 4) * PA + 2 * P : 0)>(); }
    else if (b == 4) { wait_vm<(NST > 4 ? (NST - 5) * PA + 3 * P : 0)>(); }
    else { wait_vm<(NST > 5 ? (NST - 6) * PA + 4 * P : 0)>(); }
    bar(); ia(b - 1 + NST, b - 1); ib(b - 1 + NST, b - 1); }
  int slot = NST - 1;
  for (int b = NST; b <= KT - NST; ++b) { wait_vm<(NST - 2) * P>(); bar(); ia(b - 1 + NST, slot); ib(b - 1 + NST, slot); slot = (slot + 1 == NST) ? 0 : slot + 1; }
  UNR for (int b = KT - NST + 1; b <= LAST; ++b) { const int al = KT - 1 - b;
    if (al >= 4) wait_vm<4 * P>(); else if (al == 3) wait_vm<3 * P>(); else if (al == 2) wait_vm<2 * P>(); else if (al == 1) wait_vm<P>(); else wait_vm<0>();
    bar(); }
}

template <int NP, int KS, int NST, int NSWAP>
struct Cons {
  static constexpr int MT = 4, NT = 2 * NP, STG = 16384 + NP * 8192, NR1 = MT + 2 * NT, NM1 = MT * NT, NR = NR1 * KS, NM = NM1 * KS, NADV = 4;
  static constexpr int RPS = (NR + NADV <= NM) ? 1 : 2;
  f32x4 acc[MT][NT]; bf16x8 fa[2][KS][MT]; s16x4 fb[2][KS][NT][2]; unsigned ad[4]; int slot;
  DI void init(unsigned sb, int wm, int wn, int lane) {
    const int r = lane & 15, g = lane >> 4, q = r >> 2, p4 = lane & 3, f = ((q >> 1) & 1) | ((g & 1) << 1), arow = wm * 64 + r;
    ad[0] = sb + arow * 128 + ((g ^ ((r >> 1) & 7)) * 16); ad[1] = sb + arow * 128 + (((4 + g) ^ ((r >> 1) & 7)) * 16);
    ad[2] = sb + 16384 + (8 * g + q) * 128 + (((2 * wn) ^ f) * 32) + p4 * 8; ad[3] = sb + 16384 + (8 * g + q) * 128 + (((2 * wn + 1) ^ f) * 32) + p4 * 8;
    slot = 0;
  }
  DI void init_wide(unsigned sb, int w, int lane) {
    init(sb, 0, w & 1, lane); ad[2] += (w >> 1) * 8192 - 8192; ad[3] += (w >> 1) * 8192 - 8192; }
  DI void rd1(int buf, int kb, int ks, int n) {
    if (n == 0) { fa[buf][kb][0] = *(LAS bf16x8*)(size_t)(ad[ks]); return; }
    if (n > 2 * NT) { const int i = n - 2 * NT; fa[buf][kb][i] = *(LAS bf16x8*)(size_t)(ad[ks] + i * 2048); return; }
    const int j = (n - 1) >> 1, hf = (n - 1) & 1;
    fb[buf][kb][j][hf] = ldtr(ad[2 + (j & 1)] + (j >> 1) * 8192 + ks * 4096 + hf * 512);
  }
  DI void mm1(int buf, int kb, int n, bool first) {
    const int i = n / NT, j = n % NT; const bf16x8 b = cat8(fb[buf][kb][j][0], fb[buf][kb][j][1]);
    if (j >= NSWAP) mfma_acc(acc[i][j], fa[buf][kb][i], b, first); else mfma_acc(acc[i][j], b, fa[buf][kb][i], first);
  }
  DI int next_delta() { const bool w = (slot + 1 == NST); slot = w ? 0 : slot + 1; return w ? -(NST - 1) * STG : STG; }
  DI void phase(int use, int ks0, bool rd, bool adv, int delta, bool first = false) {
    UNR for (int n = 0; n < NM; ++n) {
      mm1(use, n / NM1, n % NM1, first && n < NM1); SB();
      UNR for (int u = 0; u < RPS; ++u) { const int x = n * RPS + u;
        if (rd && x < NR) rd1(use ^ 1, x / NR1, ks0 + x / NR1, x % NR1);
        if (adv && x >= NR && x < NR + NADV) { ad[x - NR] += delta; asm volatile("" : "+v"(ad[x - NR])); } }
      SB();
    }
  }
  DI void start() {
    bar();
    UNR for (int n = 0; n < NR; ++n) rd1(0, n / NR1, n / NR1, n % NR1);
    if (KS == 2) { const int d = next_delta(); UNR for (int n = 0; n < NADV; ++n) ad[n] += d; }
    wait_lgkm0(); SB();
  }
  DI void run_half(int kt) {
    { const int d = next_delta(); phase(0, 1, true, true, d, true); wait_lgkm0(); bar(); SB(); phase(1, 0, true, false, 0); wait_lgkm0(); SB(); }
    for (int t = 1; t < kt - 1; ++t) { const int d = next_delta(); phase(0, 1, true, true, d); wait_lgkm0(); bar(); SB(); phase(1, 0, true, false, 0); wait_lgkm0(); SB(); }
    phase(0, 1, true, false, 0); wait_lgkm0(); bar(); SB(); phase(1, 0, false, false, 0); mfma_drain();
  }
  DI void run_first2() {
    bar(); SB(); { const int d = next_delta(); phase(0, 0, true, true, d, true); } wait_lgkm0();
    bar(); SB(); { const int d = next_delta(); phase(1, 0, true, true, d); } wait_lgkm0(); SB();
  }
  DI void run_pairs(int n) {
    for (int t = 0; t < n; t += 2) {
      bar(); SB(); { const int d = next_delta(); phase(0, 0, true, true, d); } wait_lgkm0();
      bar(); SB(); { const int d = next_delta(); phase(1, 0, true, true, d); } wait_lgkm0(); SB();
    }
  }
  DI void run_last2() { bar(); SB(); phase(0, 0, true, false, 0); wait_lgkm0(); SB(); phase(1, 0, false, false, 0); mfma_drain(); }
};

template <int NP2, int NC> DI f32x2 stats_comb(u32x4 (&v)[NP2]) {
  float m = 0.f; UNR for (int i = 0; i < NP2; ++i) m += __uint_as_float(v[i][0]) + __uint_as_float(v[i][2]);
  m *= 1.0f / (2 * NP2); float q = 0.f;
  UNR for (int i = 0; i < NP2; ++i) { const float d0 = __uint_as_float(v[i][0]) - m, d1 = __uint_as_float(v[i][2]) - m; q += (__uint_as_float(v[i][1]) + __uint_as_float(v[i][3])) + (float)NC * (d0 * d0 + d1 * d1); }
  return (f32x2){ln_mu(m), ln_rs(fmaxf(q, 0.f) * (1.0f / (2 * NC * NP2)))};
}

constexpr int L_MT = 139264;
DI int proj_row0() { return (((blockIdx.x & 7) >> 1) * 8 + (blockIdx.x >> 5)) * 64; }
DI int proj_col0() { return ((blockIdx.x & 1) * 4 + ((blockIdx.x >> 3) & 3)) * 128; }
struct Tab { u32x4 a[4]; };
DI void mod_load(const Prm& p, int col, Tab& q) { const __amdgpu_buffer_rsrc_t r = mkrs(p.ws + WS_MODB, 65536);
  UNR for (int k = 0; k < 4; ++k) q.a[k] = ld16_dev(r, (unsigned)(k * 4096 + col) * 4); }
DI f32x4 mod_sum(const Tab& q, bool plus1) {
  f32x4 v = (__builtin_bit_cast(f32x4, q.a[0]) + __builtin_bit_cast(f32x4, q.a[1])) + (__builtin_bit_cast(f32x4, q.a[2]) + __builtin_bit_cast(f32x4, q.a[3]));
  UNR for (int j = 0; j < 4; ++j) { v[j] = rbf(v[j]); if (plus1) v[j] = rbf(1.0f + v[j]); }
  return v;
}
DI void await_word(const Prm& p, int w) {
  for (unsigned spins = 0;;) { if (__all(__hip_atomic_load(flag_at(p, F_MB, w), RLX_AGENT) == p.tag) || give_up(p, spins)) break; }
  asm volatile("" ::: "memory");
}
template <bool LAST> DI void proj_ib(const Prm& p, unsigned sb, int lw, int lane, int t, int slot) {
  const u16* w = LAST ? p.w_out : p.wo; const int pn0 = proj_col0() >> 6;
  UNR for (int h = 0; h < 2; ++h) { const int pn = pn0 + h; const bool cp = LAST && (pn & 7) == slow_col(w);
    const int ld = cp ? 64 : D_; const char* b = (cp ? p.ws + WS_SLOW + (pn >> 3) * 229376 : (const char*)w + pn * 128) + (size_t)t * 128 * ld;
    dma2<LAST>(sb + slot * 24576 + 8192 + h * 8192 + lw * 1024, voff_b(lw, lane, ld), b, b + 64 * ld); }
}
DI void gmlp_ib0(const Prm& p, unsigned sb, int lw, int lane, int t) {
  const int cb = (blockIdx.x & 1) * 7 + (blockIdx.x >> 3) % 7; const char* b = (const char*)p.w_in + cb * 256 + (size_t)t * 64 * 3584 * 2; const unsigned vb = voff_b(lw, lane, 3584);
  issue_b<2, false>(sb + t * 49152 + lw * 1024, b + FH * 2, 3584, 64, vb); issue_b<2, false>(sb + t * 49152 + 16384 + lw * 1024, b, 3584, 64, vb);
}
template <class F> DI void publish_and_open(const Prm& p, int wave, int tid, int line, int slot, bool open, F issue) {
  bar();
  if (wave >= 4 && open) { issue(0); wait_vm<8>(); } else wait_vm<0>();
  bar();
  if (tid == 0 && line >= 0) post(p, line, slot);
  if (wave >= 4 && open) issue(1);
}
DI void softmax_pm2(f32x4 (&p)[2][2], u32x2 (&o)[2][4]) {
  { UNR for (int u = 0; u < 2; ++u) UNR for (int i = 0; i < 2; ++i) { const f32x2 a = up2(pk2v(lo2(p[u][i]))), b = up2(pk2v(hi2(p[u][i]))); p[u][i] = (f32x4){a[0], a[1], b[0], b[1]}; } }
  const float L2E = 1.4426950408889634f; float m[2], m2[2]; f32x2 a[2][4], b[2][4];
  UNR for (int u = 0; u < 2; ++u) { const f32x4 &p0 = p[u][0], &p1 = p[u][1];
    m[u] = fmaxf(fmaxf(fmaxf(fmaxf(p0[0], p0[1]), p0[2]), fmaxf(fmaxf(p0[3], p1[0]), p1[1])), fmaxf(p1[2], p1[3]));
    m2[u] = -fminf(fminf(fminf(fminf(p0[0], p0[1]), p0[2]), fminf(fminf(p0[3], p1[0]), p1[1])), fminf(p1[2], p1[3])); }
  UNR for (int u = 0; u < 2; ++u) { swap16(m[u], m2[u]); m[u] = fmaxf(m[u], m2[u]); m2[u] = m[u]; }
  UNR for (int u = 0; u < 2; ++u) { swap32(m[u], m2[u]); m[u] = fmaxf(m[u], m2[u]) * -L2E; m2[u] = m[u]; }
  UNR for (int u = 0; u < 2; ++u) swap16(m[u], m2[u]);
  SB();
  UNR for (int u = 0; u < 2; ++u) UNR for (int i = 0; i < 4; ++i) { const f32x2 v = (i & 1) ? hi2(p[u][i >> 1]) : lo2(p[u][i >> 1]); a[u][i] = v * L2E + m[u]; b[u][i] = v * -L2E + m2[u]; }
  SB();
  UNR for (int u = 0; u < 2; ++u) UNR for (int i = 0; i < 4; ++i) { a[u][i] = ex2(a[u][i]); b[u][i] = ex2(b[u][i]); }
  SB();
  UNR for (int u = 0; u < 2; ++u) { const f32x2 sa = (a[u][0] + a[u][1]) + (a[u][2] + a[u][3]), sb = (b[u][0] + b[u][1]) + (b[u][2] + b[u][3]); m[u] = sa[0] + sa[1]; m2[u] = sb[0] + sb[1]; }
  UNR for (int u = 0; u < 2; ++u) { swap16(m[u], m2[u]); m[u] += m2[u]; m2[u] = m[u]; }
  UNR for (int u = 0; u < 2; ++u) { swap32(m[u], m2[u]); m[u] = __builtin_amdgcn_rcpf(m[u] + m2[u]); m2[u] = m[u]; }
  UNR for (int u = 0; u < 2; ++u) swap16(m[u], m2[u]);
  SB();
  UNR for (int u = 0; u < 2; ++u) UNR for (int i = 0; i < 4; ++i) { a[u][i] *= m[u]; b[u][i] *= m2[u]; }
  UNR for (int u = 0; u < 2; ++u) UNR for (int i = 0; i < 2; ++i) { o[u][i] = (u32x2){pk2v(a[u][2 * i]), pk2v(a[u][2 * i + 1])}; o[u][2 + i] = (u32x2){pk2v(b[u][2 * i]), pk2v(b[u][2 * i + 1])}; }
}
DI bf16x8 cat8u(u32x2 a, u32x2 b) { return __builtin_bit_cast(bf16x8, (u32x4){a[0], a[1], b[0], b[1]}); }
DI void phase_qkv(Prm& p, LAS char* smem, int wave, int lane, int tid) {
  constexpr int NST = 3, KT = 16, L_PHI = 0, L_XKV = 34816, L_OST = 65536, L_WPHI = 122880, L_HX = 126976, L_XZ = 51200, L_KVS = 53248, L_ZS = 61440, L_TAB = 126976;
  const int xcd = blockIdx.x & 7, idx = blockIdx.x >> 3, rb = (xcd >> 2) * 8 + (idx >> 2), hp = (xcd & 3) * 4 + (idx & 3), t0 = 2 * (idx >> 2);
  const unsigned sb = (unsigned)(size_t)smem; const int r = lane & 15, g = lane >> 4, wm = (wave >> 1) & 1, wn = wave & 1;
  const int lw = wave & 3; const char* B = (const char*)p.wqkv + hp * 128; const unsigned vb = voff_b(lw, lane, 3072);
  auto ib = [&](int t, int slot) { issue_b<3, BSC>(sb + slot * 40960 + lw * 1024, B + (size_t)((t + t0) & 15) * 64 * 3072 * 2, 3072, 1024, vb); };
  auto silu = [&](u16 v) { const float c = bf_lo(v); return rbf(c * __builtin_amdgcn_rcpf(1.0f + __expf(-c))); };
  const int cbb = (blockIdx.x & 63) * 64, kb = (blockIdx.x >> 6) * 256 + ((tid & 255) >> 3); float sl2[8];
  auto fold = [&](const u32x4* w, const float* sl, int n, LAS char* red) { f32x2 a[4];
    UNR for (int i = 0; i < n; ++i) {
      UNR for (int j = 0; j < 4; ++j) { const f32x2 t = up2(w[i][j]) * sl[i]; if (i == 0) a[j] = t; else a[j] += t; } }
    UNR for (int j = 0; j < 4; ++j) UNR for (int e = 0; e < 2; ++e) { float t = a[j][e]; t += dpp<0x128>(t); a[j][e] = x16sum(t); }
    if (lane < 8) { UNR for (int j = 0; j < 4; ++j) *(LAS f32x2*)(red + (lane * 8 + 2 * j) * 4) = a[j]; }
    wait_lgkm0(); };
  {
    const int row = rb * 128 + hp * 8 + wave, b = blockIdx.x, ca = (b & 31) * 64, ka = (b >> 5) * 128 + (tid >> 3), ch = tid & 7; u32x4 xv[2], xt[2], w[2]; u16 cv[2];
    unsigned long long ep = __hip_atomic_load(flag_at(p, F_EPOCH), RLX_AGENT);
    UNR for (int i = 0; i < 2; ++i) { cv[i] = p.c[ka + 64 * i]; w[i] = __builtin_nontemporal_load((const u32x4*)(p.w_mod + (size_t)(ka + 64 * i) * 6144 + ca + ch * 8)); }
    UNR for (int i = 0; i < 2; ++i) xv[i] = __builtin_nontemporal_load((const u32x4*)(p.x + (size_t)row * D_ + i * 512 + lane * 8));
    UNR for (int i = 0; i < 2; ++i) xt[i] = __builtin_nontemporal_load((const u32x4*)(p.x + (size_t)(rb * 128 + (tid >> 3) + 64 * i) * D_ + hp * 64 + ch * 8));
    pin(ep); ++ep;
    p.tag = ((unsigned long long)(unsigned)__builtin_amdgcn_readfirstlane((unsigned)(ep >> 32)) << 32) | (unsigned)__builtin_amdgcn_readfirstlane((unsigned)ep);
    { const float sl[2] = {silu(cv[0]), silu(cv[1])}; fold(w, sl, 2, smem + wave * 256); } bar();
    const __amdgpu_buffer_rsrc_t rp = mkrs(p.ws + WS_MODA, 131072), r0 = mkrs(p.ws + WS_ST0, 262144);
    if (wave == 1 && lane < 32) { f32x2 t = {0.f, 0.f}; UNR for (int w2 = 0; w2 < 8; ++w2) t += *(LAS f32x2*)(smem + (w2 * 64 + 2 * lane) * 4);
      st16_dev(rp, (unsigned)((b >> 5) * 2048 + ca + 2 * lane) * 8, tag2(p, t[0], t[1])); }
    { f32x2 v[8]; UNR for (int j = 0; j < 8; ++j) v[j] = up2(xv[j >> 2][j & 3]);
      const f32x2 t = ((v[0] + v[1]) + (v[2] + v[3])) + ((v[4] + v[5]) + (v[6] + v[7])); const float mu = sum64(t[0] + t[1]) * (1.0f / 1024.0f); f32x2 q2 = {0.f, 0.f};
      UNR for (int j = 0; j < 8; ++j) { const f32x2 d = v[j] - mu; q2 += d * d; }
      const float sd = ln_sd(sum64(q2[0] + q2[1]) * (1.0f / 1024.0f));
      if (lane == 0) st16_dev(r0, (unsigned)row * S0SP, tag2(p, ln_mu(mu), sd)); }
    if (wave >= 4) { UNR for (int i = 0; i < 8; ++i) { sl2[i] = silu(p.c[kb + 32 * i]); pin(sl2[i]); } }
    asm volatile("" ::: "memory"); __builtin_amdgcn_s_sleep(HINT_SLEEP); asm volatile("" ::: "memory");
    if (wave < 2) {
      u32x4 g;
      for (unsigned spins = 0;;) { g = ld16_dev(r0, (unsigned)(rb * 128 + tid) * S0SP);
        if (__all(tag2_ok(p, g)) || give_up(p, spins)) break;
        asm volatile("" ::: "memory"); __builtin_amdgcn_s_sleep(RETRY_SLEEP); }
      *(LAS f32x2*)(smem + L_TAB + 512 + tid * 8) = (f32x2){tag2_val(g)[0], __builtin_amdgcn_rcpf(tag2_val(g)[1])};
    } else if (wave < 4) {
      u32x4 pa[4]; const unsigned o = (unsigned)((lane >> 5) * 4 * 2048 + (wave - 2) * 1024 + hp * 64 + 2 * (lane & 31)) * 8;
      for (unsigned spins = 0;;) { bool ok = true;
        UNR for (int k = 0; k < 4; ++k) pa[k] = ld16_dev(rp, o + k * 16384);
        UNR for (int k = 0; k < 4; ++k) ok &= tag2_ok(p, pa[k]);
        if (__all(ok) || give_up(p, spins)) break;
        asm volatile("" ::: "memory"); __builtin_amdgcn_s_sleep(RETRY_SLEEP); }
      f32x2 t = (tag2_val(pa[0]) + tag2_val(pa[1])) + (tag2_val(pa[2]) + tag2_val(pa[3]));
      UNR for (int e = 0; e < 2; ++e) { float u = t[e], v = u; swap32(u, v); u = rbf(u + v); t[e] = wave == 3 ? rbf(1.0f + u) : u; }
      if (lane < 32) *(LAS f32x2*)(smem + L_TAB + (wave - 2) * 256 + lane * 8) = t; }
    wait_lgkm0(); bar();
    { LAS char* tb = smem + L_TAB + ch * 32; const f32x4 s0 = *(LAS f32x4*)tb, s1 = *(LAS f32x4*)(tb + 16), c0v = *(LAS f32x4*)(tb + 256), c1v = *(LAS f32x4*)(tb + 272);
      const f32x2 sh[4] = {lo2(s0), hi2(s0), lo2(s1), hi2(s1)}, sc[4] = {lo2(c0v), hi2(c0v), lo2(c1v), hi2(c1v)};
      UNR for (int i = 0; i < 2; ++i) { const int r2 = (tid >> 3) + 64 * i; const f32x2 st = *(LAS f32x2*)(smem + L_TAB + 512 + r2 * 8); u32x4 o;
        UNR for (int j = 0; j < 4; ++j) o[j] = pk2v(up2(pk2v(up2(pk2v(up2(pk2v(up2(xt[i][j]) - st[0])) * st[1])) * sc[j])) + sh[j]);
        st16_dev(mkrs(p.ws + WS_H, 4u << 20), (unsigned)(((rb * 16 + hp) * 128 + r2) * 128 + ch * 16), o); } }
    wait_vm<0>(); bar();
    if (tid == 0) post(p, F_H + rb, hp);
    if (wave >= 4) { dma1(sb + L_WPHI + lw * 1024, lane * 16, (const char*)p.wphi + hp * 4096 + lw * 1024); ib(0, 0); }
    if (wave == 0) await_set(p, F_H + rb, 3u << t0, lane);
    bar();
    if (wave == 0) await(p, F_H + rb, 16, lane);
  }
  const int q = r >> 2, p4 = lane & 3; const f32x4 z4 = {0.f, 0.f, 0.f, 0.f};
  const __amdgpu_buffer_rsrc_t rk = mkrs(p.ws + WS_KVP, 2u << 20), rz = mkrs(p.ws + WS_ZP, 131072);
  LAS char* hx = smem + L_HX + lw * 4096 + lane * 16;
  bf16x8 pf[2][2];
  auto load_wf = [&](bf16x8 (&wf)[2]) { UNR for (int ft = 0; ft < 2; ++ft) { const unsigned a = sb + L_WPHI + wn * 2048 + (4 * g + q) * 64 + ft * 32 + p4 * 8; wf[ft] = cat8(ldtr(a), ldtr(a + 1024)); } };
  auto qunit = [&](const bf16x8 (&wf)[2], const bf16x8 (&qf)[2]) { f32x4 pp[2][2]; u32x2 o[2][4];
    UNR for (int u = 0; u < 2; ++u) { pp[u][0] = mfma16(wf[0], qf[u], z4); pp[u][1] = mfma16(wf[1], qf[u], z4); }
    softmax_pm2(pp, o);
    UNR for (int u = 0; u < 2; ++u) { pf[u][0] = cat8u(o[u][0], o[u][1]); pf[u][1] = cat8u(o[u][2], o[u][3]); pin(pf[u][0]); pin(pf[u][1]); } };
  if (wave >= 4) {
    const char* A = p.ws + WS_H + (size_t)rb * 16 * 16384; const unsigned va = voff_a(lw, lane, 64);
    __builtin_amdgcn_s_setprio(3);
    u32x4 w[8];
    auto wload = [&](int i) { w[i] = __builtin_nontemporal_load((const u32x4*)(p.w_mod + (size_t)(kb + 32 * i) * 6144 + 2048 + cbb + (tid & 7) * 8)); };
    auto wtouch = [&]() { const int l = blockIdx.x * 176 + lw * 44 + (lane < 44 ? lane : 0), l2 = l - 16384;
      touch_lds(sb + L_DUMMY, l2 < 0 ? (const char*)p.wo + (size_t)l * 128 : (const char*)p.w_in + (size_t)(l2 / 28) * 7168 + 3584 + (l2 % 28) * 128); };
    auto xdma = [&]() {
        const char* xs = (const char*)(p.x + (size_t)(proj_row0() + lw * 4) * D_ + proj_col0()); const size_t d = (size_t)16 * D_ * 2;
        unsigned keep; asm volatile(DMA4_STR(" nt") : "=&s"(keep) : "s"(sb + L_X1 + lw * 1024), "v"((unsigned)((lane >> 4) * D_ * 2 + (lane & 15) * 16)), "s"(xs), "s"(xs + d), "s"(xs + 2 * d), "s"(xs + 3 * d) : "memory", "scc"); };
    auto ia = [&](int t, int slot) { issue_a<true>(sb + slot * 40960 + lw * 1024, A + ((t + t0) & 15) * 16384, 64, va); };
    ia(0, 0); ib(1, 1); ia(1, 1); wait_vm<10>(); bar(); ib(2, 2); ia(2, 2);
    UNR for (int b = 1; b <= 13; ++b) { wait_vm<10>(); bar(); ia(b + 2, (b + 2) % 3); ib(b + 2, (b + 2) % 3);
      if (b == GB1) { UNR for (int i = 0; i < 4; ++i) wload(i); }
      if (b == GB2) { UNR for (int i = 4; i < 8; ++i) wload(i); }
      if (b == XB) xdma(); }
    wait_vm<10>(); bar(); wait_vm<0>(); bar();
    if (CODEPF && lw == 0) touch_code(sb + L_DUMMY, lane);
    u32x4 cpv; int cpr = blockIdx.x * 8 + (lane >> 3) + (lw == 3 ? 2048 : 0);
    const bool cpa = lw >= 2 && cpr < 3584;
    if (cpa) cpv = __builtin_nontemporal_load((const u32x4*)(p.w_out + (size_t)(cpr >= 1792 ? cpr - 1792 : cpr) * D_ + ((cpr >= 1792) * 8 + slow_col(p.w_out)) * 64 + (lane & 7) * 8));
    if (XB == 15) xdma();
    wtouch();
    if (XB == 16) xdma();
    bar();
    __builtin_amdgcn_s_setprio(0);
    bf16x8 wf[2]; load_wf(wf);
    bar();
    if (XB == 17) xdma();
    bar();
    { const bf16x8 qf[2] = {*(LAS bf16x8*)hx, *(LAS bf16x8*)(hx + 1024)}; qunit(wf, qf); }
    bar();
    if (cpa) st16_dev(mkrs(p.ws + WS_SLOW, 524288), (unsigned)(cpr * 128 + (lane & 7) * 16), cpv);
    fold(w, sl2, 8, smem + L_XZ + 1024 + lw * 256);
  } else { Cons<3, 1, NST, 4> c; c.init(sb, wm, wn, lane); c.start(); c.run_half(KT);
    bf16x8 wf[2]; load_wf(wf);
    UNR for (int mt = 0; mt < 2; ++mt) *(LAS bf16x8*)(hx + mt * 1024) = pack8(c.acc[mt][0], c.acc[mt][1]);
    wait_lgkm0(); bar();
    LAS char* phi = smem + L_PHI + wave * (64 * 136);
    __builtin_amdgcn_s_setprio(2);
    UNR for (int mt = 0; mt < 4; mt += 2) { f32x4 pp[2][2]; u32x2 o[2][4];
      UNR for (int u = 0; u < 2; ++u) { const bf16x8 kf = pack8(c.acc[mt + u][2], c.acc[mt + u][3]); pp[u][0] = mfma16(wf[0], kf, z4); pp[u][1] = mfma16(wf[1], kf, z4); }
      softmax_pm2(pp, o);
      UNR for (int u = 0; u < 2; ++u) UNR for (int ft = 0; ft < 4; ++ft) *(LAS u32x2*)(phi + (16 * (mt + u) + r) * 136 + (16 * ft + 4 * g) * 2) = o[u][ft]; }
    f32x4 kv[4][2], zz[4];
    { const u32x4 o4 = {0x3f803f80u, 0x3f803f80u, 0x3f803f80u, 0x3f803f80u}; const bf16x8 ones = __builtin_bit_cast(bf16x8, o4);
      UNR for (int kk = 0; kk < 2; ++kk) { const bf16x8 v0 = pack8(c.acc[2 * kk][4], c.acc[2 * kk + 1][4]), v1 = pack8(c.acc[2 * kk][5], c.acc[2 * kk + 1][5]);
        UNR for (int ft = 0; ft < 4; ++ft) { const unsigned a = (unsigned)(size_t)phi + (32 * kk + 4 * g + q) * 136 + ft * 32 + p4 * 8; const bf16x8 pk = cat8(ldtr(a), ldtr(a + 16 * 136));
          kv[ft][0] = mfma16(pk, v0, kk ? kv[ft][0] : z4); kv[ft][1] = mfma16(pk, v1, kk ? kv[ft][1] : z4); zz[ft] = mfma16(pk, ones, kk ? zz[ft] : z4); } } }
    LAS char* xk = smem + L_XKV + lane * 16;
    if (wm) { UNR for (int ft = 0; ft < 4; ++ft) { *(LAS f32x4*)(xk + (wave * 4 + ft) * 1024) = kv[ft][0]; kv[ft][0] = kv[ft][1]; } }
    else { UNR for (int ft = 0; ft < 4; ++ft) *(LAS f32x4*)(xk + (wave * 4 + ft) * 1024) = kv[ft][1]; }
    if (r == 0) { UNR for (int ft = 0; ft < 4; ++ft) *(LAS f32x4*)(smem + L_XZ + wave * 256 + (16 * ft + 4 * g) * 4) = zz[ft]; }
    wait_lgkm0(); bar();
    const int slotid = (2 * hp + wn) * 16 + rb;
    UNR for (int k2 = 0; k2 < 2; ++k2) { const f32x4 a = kv[2 * k2][0] + *(LAS f32x4*)(xk + ((wave ^ 2) * 4 + 2 * k2) * 1024), b = kv[2 * k2 + 1][0] + *(LAS f32x4*)(xk + ((wave ^ 2) * 4 + 2 * k2 + 1) * 1024);
      st16_dev(rk, (unsigned)(slotid * 4 + wm * 2 + k2) * 1024 + lane * 16, __builtin_bit_cast(u32x4, pack8(a, b))); }
    if (wm == 0 && lane < 16) st16_dev(rz, (unsigned)(slotid * 64 + lane * 4) * 4, __builtin_bit_cast(u32x4, *(LAS f32x4*)(smem + L_XZ + wn * 256 + lane * 16) + *(LAS f32x4*)(smem + L_XZ + (2 + wn) * 256 + lane * 16)));
    __builtin_amdgcn_s_setprio(0);
    wait_vm<0>(); bar();
    if (tid == 0) post(p, F_KV + hp, rb);
    { const bf16x8 qf[2] = {pack8(c.acc[2][0], c.acc[2][1]), pack8(c.acc[3][0], c.acc[3][1])}; qunit(wf, qf); }
    if (wave == 0) await(p, F_KV + hp, 16, lane);
  }
  bar();
  {
    const int hl = wave >> 2, ck = wave & 3, head = 2 * hp + hl;
    if (wave == 5) { float t = 0.f; UNR for (int w2 = 0; w2 < 4; ++w2) t += *(LAS float*)(smem + L_XZ + 1024 + (w2 * 64 + lane) * 4);
      st4_dev(mkrs(p.ws + WS_MODB, 65536), (unsigned)((blockIdx.x >> 6) * 4096 + cbb + lane) * 4, __float_as_uint(t)); }
    u32x4 v[16]; UNR for (int i = 0; i < 16; ++i) v[i] = ld16_dev(rk, (unsigned)((head * 16 + ((i + t0) & 15)) * 4 + ck) * 1024 + lane * 16);
    if (ck == 0) { float zs = 0.f; unsigned zv[16]; UNR for (int i = 0; i < 16; ++i) zv[i] = ld4_dev(rz, (unsigned)((head * 16 + i) * 64 + lane) * 4);
      UNR for (int i = 0; i < 16; ++i) zs += __uint_as_float(zv[i]);
      *(LAS u16*)(smem + L_ZS + hl * 128 + lane * 2) = (u16)pk2(zs, zs); }
    f32x2 a[4]; UNR for (int j = 0; j < 4; ++j) a[j] = up2(v[0][j]);
    UNR for (int i = 1; i < 16; ++i) UNR for (int j = 0; j < 4; ++j) a[j] += up2(v[i][j]);
    *(LAS u32x4*)(smem + L_KVS + hl * 4096 + ck * 1024 + lane * 16) = (u32x4){pk2v(a[0]), pk2v(a[1]), pk2v(a[2]), pk2v(a[3])};
    wait_lgkm0(); bar();
  }
  { bf16x8 kv[2][2], zf[2];
    UNR for (int dt = 0; dt < 2; ++dt) UNR for (int k2 = 0; k2 < 2; ++k2) kv[dt][k2] = *(LAS bf16x8*)(smem + L_KVS + wn * 4096 + (dt * 2 + k2) * 1024 + lane * 16);
    UNR for (int k2 = 0; k2 < 2; ++k2) { LAS char* a = smem + L_ZS + wn * 128 + (32 * k2 + 4 * g) * 2; zf[k2] = cat8(*(LAS s16x4*)a, *(LAS s16x4*)(a + 32)); }
    UNR for (int mt = 0; mt < 2; ++mt) {
      const f32x4 den = mfma16(zf[1], pf[mt][1], mfma16(zf[0], pf[mt][0], z4)); const float rd = __builtin_amdgcn_rcpf(den[0] + 1e-6f);
      UNR for (int dt = 0; dt < 2; ++dt) { const f32x4 nu = mfma16(kv[dt][1], pf[mt][1], mfma16(kv[dt][0], pf[mt][0], z4)) * rd;
        *(LAS u32x2*)(smem + L_OST + (64 * wm + (wave < 4 ? 32 : 0) + 16 * mt + r) * 144 + (32 * wn + 16 * dt + 4 * g) * 2) = (u32x2){pk2(nu[0], nu[1]), pk2(nu[2], nu[3])}; }
    }
    wait_lgkm0(); }
  bar();
  { const __amdgpu_buffer_rsrc_t ro = mkrs(p.ws + WS_O, 4u << 20);
    UNR for (int i = 0; i < 2; ++i) { const int row = (tid >> 3) + 64 * i, ch = tid & 7;
      st16_dev(ro, (unsigned)(((rb * 16 + hp) * 128 + row) * 128 + ch * 16), *(LAS u32x4*)(smem + L_OST + row * 144 + ch * 16)); } }
  publish_and_open(p, wave, tid, F_O + rb, hp, true, [&](int h) { UNR for (int t = 2 * h; t < 2 + 4 * h; ++t) proj_ib<false>(p, sb, lw, lane, t, t); });
  if (tid == 64) post(p, F_MB, blockIdx.x);
}

template <bool LAST>
DI void phase_proj(const Prm& p, LAS char* smem, int wave, int lane, int tid) {
  constexpr int NST = 6, KT = LAST ? 28 : 16, LDA = LAST ? FH : 64, KSTEP = LAST ? 128 : 16384;
  const int idx = blockIdx.x >> 3, row0 = proj_row0(), rb = row0 >> 7, cb = proj_col0() >> 7;
  const unsigned sb = (unsigned)(size_t)smem; const int r = lane & 15, g = lane >> 4;
  const int ch = tid & 15, col = cb * 128 + ch * 8; f32x4 gv[2], cv[2];
  auto tab_read = [&]() { LAS char* t = smem + L_MT + ch * 32; UNR for (int i = 0; i < 2; ++i) { gv[i] = *(LAS f32x4*)(t + 16 * i); if (!LAST) cv[i] = *(LAS f32x4*)(t + 512 + 16 * i); } wait_lgkm0(); };
  constexpr bool PG = PROG & (LAST ? 2 : 1);
  if (wave == 0) { if (PG) await_set(p, (LAST ? F_U : F_O) + rb, LAST ? 0x1u : 0x3u, lane); else await(p, (LAST ? F_U : F_O) + rb, LAST ? 14 : 16, lane); }
  bar();
  if (wave >= 4) {
    const int lw = wave - 4; const char* A = LAST ? p.ws + WS_U + (size_t)row0 * FH * 2 : p.ws + WS_O + (size_t)rb * 16 * 16384 + (row0 & 64) * 128; const unsigned va = voff_a(lw, lane, LDA);
    auto ib = [&](int t, int slot) { proj_ib<LAST>(p, sb, lw, lane, t, slot); };
    __builtin_amdgcn_s_setprio(3);
    loader_run<NST, 2, 4, KT, KT - 4>([&](int t, int slot) { dma2<true>(sb + slot * 24576 + lw * 1024, va, A + t * KSTEP, A + t * KSTEP + (size_t)64 * LDA); }, ib);
    __builtin_amdgcn_s_setprio(0);
    constexpr int X = LAST ? 0 : 2;
    if (!LAST) { const int l = blockIdx.x * 112 + lw * 28 + (lane < 28 ? lane : 0);
      touch_lds(sb + 98304, (const char*)p.w_in + (size_t)(l / 28) * 7168 + (l % 28) * 128); touch_code(sb + 98304, lane); }
    wait_vm<12 + X>(); bar(); wait_vm<6 + X>(); bar(); wait_vm<X>(); bar();
    tab_read();
  } else {
    if (PG && wave == 0) await(p, (LAST ? F_U : F_O) + rb, LAST ? 14 : 16, lane);
    Cons<1, 2, NST, 2> c; c.init_wide(sb, wave, lane); c.start(); c.run_first2();
    auto tcol = [&](int l) { return (LAST ? 3072 : (l & 32) ? 2048 : 0) + cb * 128 + (l & 31) * 4; };
    Tab tv;
    if (LAST) { if (wave == 0) mod_load(p, tcol(fresh(tid) & 63), tv); c.run_pairs(KT - 4); }
    else {
      const int l = fresh(tid) & 15, wd = (l >> 2) * 64 + (l & 2) * 16 + 2 * cb + (l & 1); unsigned long long seen = 0;
      if (wave == 0) seen = __hip_atomic_load(flag_at(p, F_MB, wd), RLX_AGENT);
      c.run_pairs(4);
      if (wave == 0) { if (!__all(seen == p.tag)) await_word(p, wd); asm volatile("" ::: "memory"); mod_load(p, tcol(fresh(tid) & 63), tv); }
      SB(); c.run_pairs(KT - 8); }
    if (wave == 0) { const int l = fresh(tid) & 63; *(LAS f32x4*)(smem + L_MT + l * 16) = mod_sum(tv, l & 32); wait_lgkm0(); SB(); }
    c.run_last2();
    tab_read();
    UNR for (int mt = 0; mt < 4; ++mt) UNR for (int j = 0; j < 2; ++j)
      *(LAS u32x2*)(smem + (16 * mt + r) * 272 + (32 * wave + 16 * j + 4 * g) * 2) = (u32x2){pk2(c.acc[mt][j][0], c.acc[mt][j][1]), pk2(c.acc[mt][j][2], c.acc[mt][j][3])};
    wait_lgkm0();
  }
  bar();
  const f32x2 g2[4] = {lo2(gv[0]), hi2(gv[0]), lo2(gv[1]), hi2(gv[1])};
  UNR for (int i = 0; i < 2; ++i) {
    const int row = (tid >> 4) + 32 * i; const u32x4 av = *(LAS u32x4*)(smem + row * 272 + ch * 16); u32x4 o;
    const u32x4 xv = *(LAS u32x4*)(smem + L_X1 + i * 8192 + tid * 16);
    UNR for (int j = 0; j < 4; ++j) o[j] = pk2v(up2(xv[j]) + up2(pk2v(g2[j] * up2(av[j]))));
    if (!LAST) *(LAS u32x4*)(smem + L_X1 + i * 8192 + tid * 16) = o;
    const size_t off = (size_t)(row0 + row) * D_ + col;
    if (LAST) *(u32x4*)(p.out + off) = o;
    else {
      const f32x2 c2[4] = {lo2(cv[0]), hi2(cv[0]), lo2(cv[1]), hi2(cv[1])}; f32x2 v[4]; u32x4 y;
      UNR for (int j = 0; j < 4; ++j) { v[j] = up2(o[j]); y[j] = pk2v(v[j] * c2[j]); }
      st16_dev(mkrs(p.ws + WS_Y, 4u << 20), (unsigned)(((rb * 16 + 2 * cb + (ch >> 3)) * 128 + (row0 & 64) + row) * 128 + (ch & 7) * 16), y);
      const f32x2 t = (v[0] + v[1]) + (v[2] + v[3]); const float mu = sum16(t[0] + t[1]) * (1.0f / 128.0f); f32x2 q = {0.f, 0.f};
      UNR for (int j = 0; j < 4; ++j) { const f32x2 d = v[j] - mu; q += d * d; }
      const float qs = sum16(q[0] + q[1]);
      if (ch == 0) st8_dev(mkrs(p.ws + WS_ST1, 131072), (unsigned)(((cb >> 1) * 2048 + row0 + row) * 16 + (cb & 1) * 8), (u32x2){__float_as_uint(mu), __float_as_uint(qs)});
    }
  }
  if (!LAST) publish_and_open(p, wave, tid, F_Y + rb, ((row0 >> 6) & 1) * 8 + cb, idx < 28, [&](int h) { gmlp_ib0(p, sb, wave & 3, lane, h); });
}

DI void helper_cs(const Prm& p, LAS char* smem, int j, int tid) {
  LAS float* tab = (LAS float*)(smem + 8192); const int ch = tid & 15, k0 = (j & 1) * 512 + (tid >> 4), wave = tid >> 6;
  const u16* wp = p.w_in + (size_t)k0 * 3584 + (j >> 1) * 128 + ch * 8; u32x4 w[16];
  { Tab q; await_word(p, (tid & 3) * 64 + 16 + 4 * wave + ((tid >> 4) & 3)); mod_load(p, 1024 + tid * 4, q);
    UNR for (int i = 0; i < 16; ++i) w[i] = *(const u32x4*)(wp + FH + (size_t)i * 32 * 3584);
    *(LAS f32x4*)(tab + tid * 4) = mod_sum(q, tid >= 256); }
  wait_lgkm0(); bar();
  for (int u = 1; u >= 0; --u) { f32x2 a[4], b[4];
    UNR for (int e = 0; e < 4; ++e) { a[e] = (f32x2){0.f, 0.f}; b[e] = a[e]; }
    UNR for (int i = 0; i < 16; ++i) { const float cc = tab[1024 + k0 + 32 * i], ss = tab[k0 + 32 * i];
      UNR for (int e = 0; e < 4; ++e) { const f32x2 v = up2(w[i][e]); a[e] += v * cc; b[e] += v * ss; } }
    LAS float* red = (LAS float*)smem;
    UNR for (int e = 0; e < 4; ++e) UNR for (int h = 0; h < 2; ++h) { a[e][h] = x16sum(a[e][h]); b[e][h] = x16sum(b[e][h]); }
    if ((tid & 63) < 16) { UNR for (int e = 0; e < 4; ++e) { *(LAS f32x2*)(red + wave * 256 + ch * 8 + 2 * e) = a[e]; *(LAS f32x2*)(red + wave * 256 + 128 + ch * 8 + 2 * e) = b[e]; } }
    wait_lgkm0(); bar();
    if (tid < 64) { f32x4 s = *(LAS f32x4*)(red + tid * 4); UNR for (int w2 = 1; w2 < 8; ++w2) s += *(LAS f32x4*)(red + w2 * 256 + tid * 4);
      st16_dev(mkrs(p.ws + WS_CSB, 65536), (unsigned)((j & 1) * 7168 + (tid >> 5) * 3584 + (14 * u + (j >> 1)) * 128 + (tid & 31) * 4) * 4, __builtin_bit_cast(u32x4, s));
      wait_vm<0>(); if (tid == 0) post(p, F_CS, 28 * u + j); }
    bar();
    if (u) { UNR for (int i = 0; i < 16; ++i) w[i] = *(const u32x4*)(wp + (size_t)i * 32 * 3584); } }
}
DI void helper_touch(const Prm& p, unsigned dummy, int hj, int wave, int tid) {
  const unsigned long long t0 = __builtin_amdgcn_s_memrealtime();
  for (int i = 0; i < 6; ++i) { int l = ((i & 3) * 32 + hj) * 512 + tid; if (i >= 4 && l >= 28672) l -= 16384;
    const char* a = i < 4 ? (const char*)p.w_sp + (size_t)(l & 2047) * 4096 + (l >> 11) * 128 : (const char*)p.w_out + (size_t)l * 128;
    while (__builtin_amdgcn_s_memrealtime() - t0 < (unsigned long long)(PACE * (i * 8 + wave))) __builtin_amdgcn_s_sleep(4);
    touch_lds(dummy, a); wait_vm<1>(); }
  wait_vm<0>();
}
DI void phase_gmlp(const Prm& p, LAS char* smem, int wave, int lane, int tid) {
  constexpr int L_IMG2 = 65536, L_IMG1 = 98304, L_TB1 = 131072, L_TB2 = 132096, L_TCS = 133120, L_ST = 135168, L_G = 0;
  const int xcd = blockIdx.x & 7, idx = blockIdx.x >> 3; const unsigned sb = (unsigned)(size_t)smem;
  if (idx >= 28) { const int j = (idx - 28) * 8 + xcd; if (j < 28) helper_cs(p, smem, j, tid);
    helper_touch(p, sb + 65536, j, wave, tid);
    publish_and_open(p, wave, tid, -1, 0, true, [&](int h) { UNR for (int t = 2 * h; t < 2 + 4 * h; ++t) proj_ib<true>(p, sb, wave & 3, lane, t, t); });
    return; }
  const int rb = (xcd >> 1) * 4 + idx / 7, cb = (xcd & 1) * 7 + idx % 7;
  auto chunk_at = [&](int base, int row, int ch) { return (LAS u32x4*)(smem + base + row * 256 + ((ch ^ (row & 15)) * 16)); };
  auto bars = [&](int n) { for (int i = 0; i < n; ++i) bar(); };
  auto normalize = [&]() {
    const int tid = fresh(threadIdx.x); const __amdgpu_buffer_rsrc_t ro = mkrs(p.ws + WS_Z2N, 8u << 20); u32x4 v[4]; f32x2 st[4];
    UNR for (int i = 0; i < 4; ++i) { const int row = 32 * i + (tid >> 4); v[i] = *chunk_at(L_IMG2, row, tid & 15); st[i] = *(LAS f32x2*)(smem + L_TB2 + row * 8); }
    UNR for (int i = 0; i < 4; ++i) { UNR for (int e = 0; e < 4; ++e) v[i][e] = pk2v(up2(v[i][e]) * st[i][0] + st[i][1]);
      st16_dev(ro, (unsigned)((rb * 128 + 32 * i + (tid >> 4)) * FH + cb * 128 + (tid & 15) * 8) * 2, v[i]); }
    wait_vm<0>(); };
  float bs[4];
  if (wave >= 6) {
    const int row = tid - 384; const unsigned so = (unsigned)(rb * 128 + row) * 16;
    bar();
    f32x2 tb1; f32x4 tcs;
    { u32x4 sv[4]; const __amdgpu_buffer_rsrc_t rs = mkrs(p.ws + WS_ST1, 131072);
      wait_vm<0>(); bar(); if (PROG & 4) await(p, F_Y + rb, 16, lane);
      UNR for (int i = 0; i < 4; ++i) sv[i] = ld16_dev(rs, so + i * 32768);
      bars(2); tb1 = stats_comb<4, 128>(sv); pin(tb1); }
    bars(11);
    { const int u = 7 - wave; const int wd = 256 + 28 * u + 2 * cb + (lane & 1); const unsigned long long s1 = __hip_atomic_load(flag_at(p, F_MB, wd), RLX_AGENT); bar(); if (!__all(s1 == p.tag)) await_word(p, wd); asm volatile("" ::: "memory");
      const __amdgpu_buffer_rsrc_t rc = mkrs(p.ws + WS_CSB, 65536); const unsigned o = (unsigned)(u * FH + cb * 128 + (lane >> 5) * 3584 + (lane & 31) * 4) * 4;
      const u32x4 c0 = ld16_dev(rc, o), c1 = ld16_dev(rc, o + 7168 * 4); bar();
      tcs = __builtin_bit_cast(f32x4, c0) + __builtin_bit_cast(f32x4, c1); pin(tcs); }
    bar();
    *(LAS f32x2*)(smem + L_TB1 + row * 8) = tb1; *(LAS f32x4*)(smem + L_TCS + row * 16) = tcs; wait_lgkm0();
    bars(2);
    __builtin_amdgcn_s_setprio(3);
    { const __amdgpu_buffer_rsrc_t rs = mkrs(p.ws + WS_ST2, 14 * 2048 * 16);
      const f32x4 s4 = *(LAS f32x4*)(smem + L_ST + row * 16); const float d = s4[0] - s4[2];
      st16_dev(rs, cb * 32768 + so, tag2(p, 0.5f * (s4[0] + s4[2]), s4[1] + s4[3] + 32.0f * d * d));
      __builtin_amdgcn_s_sleep(S2_SLEEP);
      u32x4 sv[14];
      for (unsigned spins = 0;;) { bool ok = true;
        UNR for (int i = 0; i < 14; ++i) sv[i] = ld16_dev(rs, so + i * 32768);
        UNR for (int i = 0; i < 14; ++i) ok &= tag2_ok(p, sv[i]);
        if (__all(ok) || give_up(p, spins)) break;
        asm volatile("" ::: "memory"); __builtin_amdgcn_s_sleep(4); }
      float m = 0.f, q = 0.f;
      UNR for (int i = 0; i < 14; ++i) m += __uint_as_float(sv[i][0] << 8);
      m *= 1.0f / 14.0f;
      UNR for (int i = 0; i < 14; ++i) { const float e = __uint_as_float(sv[i][0] << 8) - m; q += __uint_as_float(sv[i][2] << 8) + 128.0f * e * e; }
      const float rs2 = ln_rs(fmaxf(q, 0.f) * (1.0f / 1792.0f)); *(LAS f32x2*)(smem + L_TB2 + row * 8) = (f32x2){rs2, -ln_mu(m) * rs2}; wait_lgkm0(); }
    __builtin_amdgcn_s_setprio(0); bar(); normalize(); bar();
    bars(2); if ((PROG & 8) && wave == 7) await(p, F_Z + cb, 16, lane);
    bars(24);
    if (CODEPF && wave == 6) touch_code(sb + L_TB1, lane);
    bars(7);
    UNR for (int i = 0; i < 4; ++i) bs[i] = bf_lo(p.b_sp[rb * 128 + 32 * i + (tid >> 4)]);
  } else if (wave >= 4) {
    const int lw = wave - 4; const unsigned s0 = sb + lw * 1024;
    { const char* A = p.ws + WS_Y + (size_t)rb * 16 * 16384; const char* B = (const char*)p.w_in + cb * 256; const unsigned va = voff_a(lw, lane, 64), vb = voff_b(lw, lane, 3584);
      auto ia = [&](int t, int slot) { const unsigned s = s0 + slot * 49152; const char* a = A + t * 16384; dma4h<true>(s, va, a, 2048); dma4h<true>(s + 8192, va, a + 8192, 2048); };
      auto ib = [&](int t, int slot) { const unsigned s = s0 + slot * 49152 + 16384; const char* b = B + (size_t)t * 64 * 3584 * 2;
        dma4h<BSC>(s, vb, b + FH * 2, 32 * 3584); dma4h<BSC>(s + 8192, vb, b + FH * 2 + 128, 32 * 3584); dma4h<BSC>(s + 16384, vb, b, 32 * 3584); dma4h<BSC>(s + 24576, vb, b + 128, 32 * 3584); };
      bar();
      __builtin_amdgcn_s_setprio(3);
      ia(0, 0); ia(1, 1); wait_vm<8>(); bar();
      ia(2, 2); ib(2, 2); int slot = 0;
      for (int b = 1; b <= 13; ++b) { wait_vm<24>(); bar(); ia(b + 2, slot); ib(b + 2, slot); slot = (slot == 2) ? 0 : slot + 1; }
      wait_vm<24>(); bar(); wait_vm<0>(); bar(); bar(); }
    const char* A = (const char*)p.w_sp + (size_t)rb * 128 * S_ * 2; const char* B = p.ws + WS_Z2N + cb * 256; const unsigned va = voff_a(lw, lane, S_), vb = voff_b(lw, lane, FH);
    auto ia = [&](int t, int slot) { const unsigned s = s0 + slot * 32768; const char* a = A + t * 128; dma4h<BSC>(s, va, a, 32 * S_); dma4h<BSC>(s + 8192, va, a + 128 * S_, 32 * S_); };
    auto ib = [&](int t, int slot) { const unsigned s = s0 + slot * 32768 + 16384; const char* b = B + (size_t)t * 64 * FH * 2; dma4h<true>(s, vb, b, 32 * FH); dma4h<true>(s + 8192, vb, b + 128, 32 * FH); };
    __builtin_amdgcn_s_setprio(0);
    ia(0, 0); ia(1, 1);
    bars(3); normalize(); bar();
    ia(2, 2);
    bar();
    __builtin_amdgcn_s_setprio(3);
    ib(0, 0); ib(1, 1); wait_vm<8>(); bar();
    ib(2, 2); wait_vm<8>(); bar(); ia(3, 0); ib(3, 0); int slot = 1;
    for (int b = 2; b <= 29; ++b) { wait_vm<16>(); bar(); ia(b + 2, slot); ib(b + 2, slot); slot = (slot == 2) ? 0 : slot + 1; }
    wait_vm<16>(); bar(); wait_vm<0>(); bar();
    __builtin_amdgcn_s_setprio(0);
    UNR for (int i = 0; i < 4; ++i) bs[i] = bf_lo(p.b_sp[rb * 128 + 32 * i + (tid >> 4)]);
  } else {
    const int wm = wave >> 1, wn = wave & 1;
    auto frag_at = [&](int base, int mt, int j, int r, int g) { const int row = 64 * wm + 16 * mt + r, ch = 8 * (j >> 1) + 4 * wn + 2 * (j & 1) + (g >> 1); return (LAS u32x2*)(smem + base + row * 256 + ((ch ^ (row & 15)) * 16) + (g & 1) * 8); };
    { Cons<4, 1, 3, 8> c; c.init(sb, wm, wn, lane);
      auto leave = [&](bool z2) {
        f32x4 cs[4], bi[4], sm[4], sq[4]; f32x2 st[4]; const int l2 = fresh(threadIdx.x), r = l2 & 15, g = (l2 >> 4) & 3, j0 = z2 ? 0 : 4;
        UNR for (int j = 0; j < 4; ++j) { LAS char* t = smem + L_TCS + (z2 ? 0 : 1024) + (64 * (j >> 1) + 32 * wn + 16 * (j & 1) + 4 * g) * 4; cs[j] = *(LAS f32x4*)t; bi[j] = *(LAS f32x4*)(t + 512); }
        UNR for (int mt = 0; mt < 4; ++mt) st[mt] = *(LAS f32x2*)(smem + L_TB1 + (64 * wm + 16 * mt + r) * 8);
        UNR for (int mt = 0; mt < 4; ++mt) { f32x2 z[8], w[8];
          UNR for (int j = 0; j < 4; ++j) { z[2 * j] = lo2(c.acc[mt][j0 + j]) - lo2(cs[j]) * st[mt][0]; z[2 * j + 1] = hi2(c.acc[mt][j0 + j]) - hi2(cs[j]) * st[mt][0]; }
          SB();
          UNR for (int j = 0; j < 4; ++j) { z[2 * j] = z[2 * j] * st[mt][1] + lo2(bi[j]); z[2 * j + 1] = z[2 * j + 1] * st[mt][1] + hi2(bi[j]); }
          SB();
          { const float k1 = -2.0f * 0.7978845608028654f * 1.4426950408889634f, k3 = k1 * 0.044715f;
            UNR for (int e = 0; e < 8; ++e) w[e] = z[e] * z[e];
            SB(); UNR for (int e = 0; e < 8; ++e) w[e] = w[e] * k3 + k1;
            SB(); UNR for (int e = 0; e < 8; ++e) w[e] *= z[e];
            SB(); UNR for (int e = 0; e < 8; ++e) w[e] = ex2(w[e]);
            SB(); UNR for (int e = 0; e < 8; ++e) w[e] += 1.0f;
            SB(); UNR for (int e = 0; e < 8; ++e) w[e] = (f32x2){__builtin_amdgcn_rcpf(w[e][0]), __builtin_amdgcn_rcpf(w[e][1])};
            SB(); UNR for (int e = 0; e < 8; ++e) { z[e] *= w[e]; pin(z[e]); }
            SB(); }
          u32x2 zp[4];
          UNR for (int j = 0; j < 4; ++j) { zp[j] = (u32x2){pk2v(z[2 * j]), pk2v(z[2 * j + 1])}; *frag_at(z2 ? L_IMG2 : L_IMG1, mt, j, r, g) = zp[j]; }
          if (z2) {
            const bf16x8 f0 = cat8u(zp[0], zp[1]), f1 = cat8u(zp[2], zp[3]), ones = __builtin_bit_cast(bf16x8, (u32x4){0x3f803f80u, 0x3f803f80u, 0x3f803f80u, 0x3f803f80u}); const f32x4 z4 = {0.f, 0.f, 0.f, 0.f};
            sm[mt] = mfma16(ones, f1, mfma16(ones, f0, z4)); sq[mt] = mfma16(f1, f1, mfma16(f0, f0, z4)); } }
        if (z2) { UNR for (int mt = 0; mt < 4; ++mt) { const float ss = (r & 2) ? ((r & 1) ? sq[mt][3] : sq[mt][2]) : ((r & 1) ? sq[mt][1] : sq[mt][0]), m = sm[mt][0] * (1.0f / 64.0f);
            if (g == (r >> 2)) *(LAS f32x2*)(smem + L_ST + (64 * wm + 16 * mt + r) * 16 + wn * 8) = (f32x2){m, ss - sm[mt][0] * m}; } }
        wait_lgkm0(); SB(); };
      if (wave == 0) { if (PROG & 4) await_set(p, F_Y + rb, 0x0303u, lane); else await(p, F_Y + rb, 16, lane); }
      bar(); c.start(); c.run_half(16);
      bar(); leave(true); bar();
      leave(false); }
    bar(); normalize(); bar();
    if (tid == 0) post(p, F_Z + cb, rb);
    if (wave == 0) { if (PROG & 8) await_set(p, F_Z + cb, 0x3u, lane); else await(p, F_Z + cb, 16, lane); }
    bar();
    Cons<2, 2, 3, 4> c; c.init(sb, wm, wn, fresh(threadIdx.x) & 63); c.start();
    c.run_first2(); c.run_pairs(28);
    { const int t2 = fresh(threadIdx.x); UNR for (int i = 0; i < 4; ++i) bs[i] = bf_lo(p.b_sp[rb * 128 + 32 * i + (t2 >> 4)]); }
    c.run_last2();
    const int l2 = fresh(threadIdx.x);
    UNR for (int mt = 0; mt < 4; ++mt) UNR for (int j = 0; j < 4; ++j) *frag_at(L_G, mt, j, l2 & 15, (l2 >> 4) & 3) = (u32x2){pk2(c.acc[mt][j][0], c.acc[mt][j][1]), pk2(c.acc[mt][j][2], c.acc[mt][j][3])};
    wait_lgkm0();
  }
  bar();
  { const __amdgpu_buffer_rsrc_t ro = mkrs(p.ws + WS_U, 8u << 20); const int tid = fresh(threadIdx.x);
    UNR for (int i = 0; i < 4; ++i) { const int row = 32 * i + (tid >> 4), ch = tid & 15; const u32x4 z = *chunk_at(L_IMG1, row, ch), gg = *chunk_at(L_G, row, ch); u32x4 o;
      UNR for (int e = 0; e < 4; ++e) o[e] = pk2v(up2(z[e]) * (up2(gg[e]) + bs[i]));
      st16_dev(ro, (unsigned)((rb * 128 + row) * FH + cb * 128 + ch * 8) * 2, o); } }
  publish_and_open(p, wave, tid, F_U + rb, cb, true, [&](int h) { UNR for (int t = 2 * h; t < 2 + 4 * h; ++t) proj_ib<true>(p, sb, wave & 3, lane, t, t); });
}

__global__ __launch_bounds__(512, 2) void k_main(Args args) {
  extern __shared__ __attribute__((aligned(16))) char smem_[];
  LAS char* smem = (LAS char*)smem_;
  const int tid = threadIdx.x, lane = tid & 63, wave = __builtin_amdgcn_readfirstlane(tid >> 6);
  Prm p; (Args&)p = args;
  asm volatile(".Lkm_begin:");
  if (wave == 2) touch_code((unsigned)(size_t)smem + L_DUMMY, lane);
  phase_qkv(p, smem, wave, lane, tid);
  phase_proj<false>(p, smem, wave, fresh(tid) & 63, fresh(tid));
  phase_gmlp(p, smem, wave, fresh(tid) & 63, fresh(tid));
  phase_proj<true>(p, smem, wave, fresh(tid) & 63, fresh(tid));
  if (blockIdx.x == 0 && threadIdx.x == 0) __hip_atomic_store(flag_at(p, F_EPOCH), p.tag, RLX_AGENT);
  asm volatile(".Lkm_end:");
}

extern "C" void kernel_launch(void* const* d_in, const int* in_sizes, int n_in, void* d_out, int out_size, void* d_ws, size_t ws_size, hipStream_t stream) {
  Args p;
  p.x = (const u16*)d_in[0]; p.wqkv = (const u16*)d_in[1]; p.wphi = (const u16*)d_in[2]; p.wo = (const u16*)d_in[3];
  p.w_in = (const u16*)d_in[5]; p.w_sp = (const u16*)d_in[6]; p.b_sp = (const u16*)d_in[7]; p.w_out = (const u16*)d_in[8];
  p.c = (const u16*)d_in[9]; p.w_mod = (const u16*)d_in[10]; p.out = (u16*)d_out; p.ws = (char*)d_ws;
  (void)hipFuncSetAttribute((const void*)k_main, hipFuncAttributeMaxDynamicSharedMemorySize, 163840);
  k_main<<<256, 512, 163840, stream>>>(p);
}
```
